```python
import torch

def _softmax(x, axis=-1):
    return torch.softmax(x, dim=axis)
def _gelu(x):
    return torch.nn.functional.gelu(x, approximate="tanh")
def _silu(x):
    return torch.nn.functional.silu(x)
def _sigmoid(x):
    return torch.sigmoid(x)
def _layernorm(x, axis=-1, eps=1e-5):
    m = x.mean(dim=axis, keepdim=True)
    v = x.var(dim=axis, keepdim=True, unbiased=False)
    return (x - m) / torch.sqrt(v + eps)
def _scatter_add(tgt, idx, src, axis=0):
    return tgt.clone().index_add_(axis, idx, src)
def _segment_sum(src, seg, n):
    out = torch.zeros((n,) + src.shape[1:], dtype=src.dtype)
    return out.index_add_(0, seg, src)
def _pad(x, pw):
    flat = tuple(v for pair in reversed(pw) for v in pair)
    return torch.nn.functional.pad(x, flat)

kernel_name = 'interleave_layers__spectral_gfnet__grouped_kv_mqa__prompt'

def setup_inputs():

    _w = (lambda a: (lambda t: t.to(torch.bfloat16) if t.is_floating_point() else t)(torch.as_tensor(a)))
    def _A_setup():
        B, S, D = 1, 2048, 512
        Df = D // 2 + 1
        return {
            "x": _w((lambda *s: torch.randn(*s, dtype=torch.bfloat16) * 0.05)(B, S, D)),
            "w_r": _w((lambda *s: torch.randn(*s, dtype=torch.bfloat16) * 0.8)(S, Df) * 0.02),
            "w_i": _w((lambda *s: torch.randn(*s, dtype=torch.bfloat16) * 0.8)(S, Df) * 0.02),
        }
    _A_in = _A_setup()

    def _B_setup():
        B, S, D, H = 1, 2048, 512, 32
        Dh = D // H
        return {
            "x": _w((lambda *s: torch.randn(*s, dtype=torch.bfloat16) * 0.05)(B, S, D)),
            "wq": _w((lambda *s: torch.randn(*s, dtype=torch.bfloat16) * 0.05)(D, H * Dh)),
            "wk": _w((lambda *s: torch.randn(*s, dtype=torch.bfloat16) * 0.05)(D, Dh)),
            "wv": _w((lambda *s: torch.randn(*s, dtype=torch.bfloat16) * 0.05)(D, Dh)),
            "wo": _w((lambda *s: torch.randn(*s, dtype=torch.bfloat16) * 0.05)(H * Dh, D)),
        }
    _B_in = _B_setup()

    return {
        "A_x": _A_in["x"],
        "A_w_r": _A_in["w_r"],
        "A_w_i": _A_in["w_i"],
        "B_x": _B_in["x"],
        "B_wq": _B_in["wq"],
        "B_wk": _B_in["wk"],
        "B_wv": _B_in["wv"],
        "B_wo": _B_in["wo"],
    }

def reference(A_x, A_w_r, A_w_i, B_x, B_wq, B_wk, B_wv, B_wo):
    def _A_ref(x, w_r, w_i):
        B, S, D = x.shape
        X = (lambda x, axes=(-2,-1): torch.fft.rfft2(x.float(), dim=axes))(x, axes=(-2, -1))
        W = w_r + 1j * w_i
        X = X * W
        y = (lambda x, s, axes=(-2,-1): torch.fft.irfft2(x, s=s, dim=axes).to(torch.bfloat16))(X, (S, D), axes=(-2, -1))
        return y

    def _B_ref(x, wq, wk, wv, wo):
        B, S, D = x.shape
        H = 32
        Dh = D // H
        q = (lambda x, *d: x.permute(*d))((x @ wq).reshape(B, S, H, Dh), 0, 2, 1, 3)
        k = x @ wk
        v = x @ wv
        scores = torch.einsum("bhqd,bkd->bhqk", q, k) * (Dh ** -0.5)
        p = _softmax(scores, axis=-1)
        o = torch.einsum("bhqk,bkd->bhqd", p, v)
        return (lambda x, *d: x.permute(*d))(o, 0, 2, 1, 3).reshape(B, S, H * Dh) @ wo

    x = A_x
    x = x + _A_ref(_layernorm(x, axis=-1), A_w_r, A_w_i)
    x = x + _B_ref(_layernorm(x, axis=-1), B_wq, B_wk, B_wv, B_wo)
    x = x + _A_ref(_layernorm(x, axis=-1), A_w_r, A_w_i)
    x = x + _B_ref(_layernorm(x, axis=-1), B_wq, B_wk, B_wv, B_wo)
    return x
```

```cpp
#include <hip/hip_runtime.h>
#include <stdint.h>

typedef float f32x2 __attribute__((ext_vector_type(2)));
typedef float f32x4 __attribute__((ext_vector_type(4)));
typedef float f32x8 __attribute__((ext_vector_type(8)));
typedef float f32x16 __attribute__((ext_vector_type(16)));
typedef __bf16 bf16x2 __attribute__((ext_vector_type(2)));
typedef __bf16 bf16x8 __attribute__((ext_vector_type(8)));
typedef __bf16 bf16x16 __attribute__((ext_vector_type(16)));
typedef short s16x4 __attribute__((ext_vector_type(4)));
typedef unsigned u32x2 __attribute__((ext_vector_type(2)));
typedef unsigned u32x4 __attribute__((ext_vector_type(4)));
typedef unsigned u32x8 __attribute__((ext_vector_type(8)));
typedef unsigned short u16;

#define DEVI __device__ __forceinline__
#define SB() __builtin_amdgcn_sched_barrier(0)

constexpr size_t OFF_U   = 0;
constexpr size_t OFF_UP  = OFF_U + (2u << 20);
constexpr size_t OFF_X1  = OFF_UP + (2u << 20);
constexpr size_t OFF_LN  = OFF_X1 + (2u << 20);
constexpr size_t OFF_WT  = OFF_LN + (2u << 20);
constexpr size_t OFF_WQ  = OFF_WT + (4u << 20);
constexpr size_t OFF_WO  = OFF_WQ + (512u << 10);
constexpr size_t OFF_WKV = OFF_WO + (512u << 10);
constexpr size_t OFF_KV  = OFF_WKV + (32u << 10);
constexpr int KV_BYTES = 65536 * 2 + 8192;

constexpr float GAM = 145.0f / 512.0f;
constexpr float TWS = (128.0f / 145.0f) * (128.0f / 145.0f);

DEVI unsigned pack2(float a, float b) { bf16x2 v = {(__bf16)a, (__bf16)b}; return __builtin_bit_cast(unsigned, v); }
DEVI float bflo(unsigned u) { return __builtin_bit_cast(float, u << 16); }
DEVI float bfhi(unsigned u) { return __builtin_bit_cast(float, u & 0xffff0000u); }
DEVI float sinr(float rev) { return __builtin_amdgcn_sinf(rev); }
DEVI float cosr(float rev) { return __builtin_amdgcn_cosf(rev); }
DEVI f32x4 mf32(u32x4 a, u32x4 b, f32x4 c) {
  return __builtin_amdgcn_mfma_f32_16x16x32_bf16(__builtin_bit_cast(bf16x8, a), __builtin_bit_cast(bf16x8, b), c, 0, 0, 0);
}
DEVI f32x4 mf16(u32x2 a, u32x2 b, f32x4 c) {
  return __builtin_amdgcn_mfma_f32_16x16x16bf16_1k(__builtin_bit_cast(s16x4, a), __builtin_bit_cast(s16x4, b), c, 0, 0, 0);
}
DEVI f32x4 smf64(u32x4 a, u32x8 b, f32x4 c, int idx) {
  return __builtin_amdgcn_smfmac_f32_16x16x64_bf16(__builtin_bit_cast(bf16x8, a), __builtin_bit_cast(bf16x16, b), c, idx, 0, 0);
}
DEVI u32x4 pack4(f32x4 re, f32x4 im) { return u32x4{pack2(re[0], im[0]), pack2(re[1], im[1]), pack2(re[2], im[2]), pack2(re[3], im[3])}; }

template <int CTRL> DEVI float dppadd(float x) {
  return x + __builtin_bit_cast(float, __builtin_amdgcn_update_dpp(0, __builtin_bit_cast(int, x), CTRL, 0xf, 0xf, true));
}
DEVI float wave_sum(float x) {
  x = dppadd<0xB1>(x);
  x = dppadd<0x4E>(x);
  x = dppadd<0x141>(x);
  x = dppadd<0x140>(x);
  float r0 = __builtin_bit_cast(float, __builtin_amdgcn_readlane(__builtin_bit_cast(int, x), 0));
  float r1 = __builtin_bit_cast(float, __builtin_amdgcn_readlane(__builtin_bit_cast(int, x), 16));
  float r2 = __builtin_bit_cast(float, __builtin_amdgcn_readlane(__builtin_bit_cast(int, x), 32));
  float r3 = __builtin_bit_cast(float, __builtin_amdgcn_readlane(__builtin_bit_cast(int, x), 48));
  return (r0 + r1) + (r2 + r3);
}

struct Dft { u32x4 re, im; };
struct Tw { f32x4 c, s; };

DEVI Dft make_dft(int l, float sgn, int shift) {
  Dft d; int k = (l & 15) + shift, q = l >> 4;
#pragma unroll
  for (int r = 0; r < 4; ++r) {
    float rev = (float)(((4 * q + r) * k) & 15) * (1.0f / 16.0f);
    float c = GAM * cosr(rev), s = sgn * GAM * sinr(rev);
    d.re[r] = pack2(c, -s); d.im[r] = pack2(s, c);
  }
  return d;
}
DEVI Tw make_tw(int l, float sgn, int shift) {
  Tw t; int k = (l & 15) + shift, q = l >> 4;
#pragma unroll
  for (int r = 0; r < 4; ++r) {
    float rev = (float)((4 * q + r) * k) * (1.0f / 256.0f);
    t.c[r] = TWS * cosr(rev); t.s[r] = sgn * TWS * sinr(rev);
  }
  return t;
}
DEVI void fft256(u32x4 a, const Dft& d1, const Tw& t, const Dft& d2, f32x4& ore, f32x4& oim) {
  f32x4 z = {0, 0, 0, 0};
  f32x4 re = mf32(a, d1.re, z), im = mf32(a, d1.im, z);
  f32x4 r2 = re * t.c - im * t.s, i2 = re * t.s + im * t.c;
  u32x4 p = pack4(r2, i2);
  ore = mf32(d2.re, p, ore); oim = mf32(d2.im, p, oim);
}
DEVI int kappa(int idx) { return idx == 6 ? 0 : (idx == 7 ? 4 : ((idx & 1) ? -((idx >> 1) + 1) : ((idx >> 1) + 1))); }
DEVI int pidx_of_block(int b) { return (b & 7) * 32 + (b >> 3); }
DEVI int s2_of_pidx(int p) { int r = p & 3, lb = p >> 2; return 16 * (4 * (lb >> 4) + r) + (lb & 15); }

template <bool INV> DEVI void make_r8(int l, int s2, u32x2& m1, u32x2& m2) {
  int row = l & 15, q = l >> 4, co = row & 1;
#pragma unroll
  for (int e = 0; e < 2; ++e) {
    int a = row >> 1, b = 2 * q + e;
    int idx = INV ? b : a, j = INV ? a : b;
    int n = ((s2 + 256 * j) * kappa(idx)) & 2047;
    float rev = (float)n * (1.0f / 2048.0f);
    float c = 0.35355339059f * cosr(rev), s = (INV ? 0.35355339059f : -0.35355339059f) * sinr(rev);
    m1[e] = co ? pack2(s, c) : pack2(c, -s);
    m2[e] = co ? pack2(-s, c) : pack2(c, s);
  }
}

DEVI u32x4 layernorm_pack(f32x4 re, f32x4 im) {
  f32x4 s4 = re + im;
  float m = wave_sum((s4[0] + s4[1]) + (s4[2] + s4[3])) * (1.0f / 512.0f);
  re -= m; im -= m;
  f32x4 q4 = re * re + im * im;
  float v = wave_sum((q4[0] + q4[1]) + (q4[2] + q4[3])) * (1.0f / 512.0f);
  float rs = __builtin_amdgcn_rsqf(v + 1e-5f);
  return pack4(re * rs, im * rs);
}

DEVI void phaseA(f32x4 re, f32x4 im, int s2, unsigned* lds, u32x4* urec, int t) {
  int l = t & 63, w = t >> 6, c16 = l & 15, q = l >> 4;
  Dft F = make_dft(l, -1.f, 0); Tw T = make_tw(l, -1.f, 0);
  u32x2 m1, m2; make_r8<false>(l, s2, m1, m2);
  u32x4 a = layernorm_pack(re, im);
  f32x4 ore = {0, 0, 0, 0}, oim = {0, 0, 0, 0};
  fft256(a, F, T, F, ore, oim);
  u32x4 p = pack4(ore, oim);
#pragma unroll
  for (int r = 0; r < 4; ++r) lds[(c16 + 16 * (4 * q + r)) * 10 + w] = p[r];
  __syncthreads();
  int sg = 16 * w + c16, g2 = sg ? 256 - sg : 128;
  u32x2 b1 = *(const u32x2*)(lds + sg * 10 + 2 * q), b2 = *(const u32x2*)(lds + g2 * 10 + 2 * q);
  f32x4 z = {0, 0, 0, 0};
  f32x4 e1 = mf16(m1, b1, z), e2 = mf16(m2, b2, z);
  urec[t] = u32x4{pack2(e1[0], e1[1]), pack2(e1[2], e1[3]), pack2(e2[0], e2[1]), pack2(e2[2], e2[3])};
}

struct TileInfo { int k1, g, partner, shift; };
DEVI TileInfo tile_info(int cb, int w) {
  TileInfo ti; int half = w >> 2, i = w & 3;
  int q = (cb >> 3) & 3, sg = 16 * (cb >> 5) + 2 * (cb & 7) + half;
  int kp = kappa(2 * q + (i & 1));
  ti.k1 = (i & 2) ? -kp : kp;
  ti.g = (i & 2) ? (sg ? 256 - sg : 128) : sg;
  ti.partner = w ^ 2; ti.shift = 0;
  if (sg == 0) {
    if (q < 3) ti.partner = w ^ 1;
    else { ti.partner = w; ti.shift = (i == 1) ? 1 : (i == 3 ? -1 : 0); }
  }
  return ti;
}

__global__ void __launch_bounds__(512) k_prep(const u16* __restrict__ wr, const u16* __restrict__ wi, const u16* __restrict__ wq,
                                              const u16* __restrict__ wk, const u16* __restrict__ wv, const u16* __restrict__ wo, char* ws) {
  int b = blockIdx.x, t = threadIdx.x, l = t & 63, w = t >> 6;
  {
    TileInfo ti = tile_info(b, w);
    u32x4 A, B;
#pragma unroll
    for (int r = 0; r < 4; ++r) {
      int k2 = (l & 15) + 16 * (4 * (l >> 4) + r);
      int k = (ti.k1 + 8 * k2) & 2047, kn = (-k) & 2047;
      if (ti.g) {
        A[r] = wr[k * 257 + ti.g] | ((unsigned)wi[k * 257 + ti.g] << 16);
        B[r] = wr[kn * 257 + 256 - ti.g] | (((unsigned)wi[kn * 257 + 256 - ti.g] ^ 0x8000u) << 16);
      } else {
        A[r] = pack2(0.5f * (bflo(wr[k * 257]) + bflo(wr[kn * 257])), 0.5f * (bflo(wi[k * 257]) - bflo(wi[kn * 257])));
        B[r] = pack2(0.5f * (bflo(wr[k * 257 + 256]) + bflo(wr[kn * 257 + 256])), 0.5f * (bflo(wi[k * 257 + 256]) - bflo(wi[kn * 257 + 256])));
      }
    }
    u32x4* wt = (u32x4*)(ws + OFF_WT) + (size_t)b * 1024;
    wt[t] = A; wt[512 + t] = B;
  }
  if (t < 384) {
    int which = t >> 7, half = (t >> 6) & 1, n = l & 15, g = l >> 4;
    if (which == 2 && b >= 16) return;
    u16 v[8];
#pragma unroll
    for (int s7 = 0; s7 < 8; ++s7) {
      int p = (s7 >> 1) & 1, jh = 2 * (g & 1) + (s7 >> 2), jl = s7 & 1, ga = 2 * half + (g >> 1);
      if (which == 1) {
        int qt = b >> 5, wv_ = (b >> 2) & 7, i = b & 3;
        int j = 2 * jh + jl, d = (j & 3) + 8 * (j >> 2) + 4 * half;
        int row = (4 * qt + p + 2 * (g >> 1)) * 16 + d, col = 128 * (wv_ >> 1) + 32 * i + 16 * (wv_ & 1) + n;
        v[s7] = wo[row * 512 + col];
      } else {
        int ks = b & 7, hd = b >> 3;
        int ll = ks * 8 + ga * 2 + p;
        int d = 2 * (16 * (4 * (ll >> 4) + jh) + (ll & 15)) + jl;
        v[s7] = which == 0 ? wq[d * 512 + hd * 16 + n] : (hd == 0 ? wk[d * 16 + n] : wv[d * 16 + n]);
      }
    }
    u32x4 o = {v[0] | ((unsigned)v[1] << 16), v[2] | ((unsigned)v[3] << 16), v[4] | ((unsigned)v[5] << 16), v[6] | ((unsigned)v[7] << 16)};
    size_t base = which == 0 ? OFF_WQ : (which == 1 ? OFF_WO : OFF_WKV);
    ((u32x4*)(ws + base))[(size_t)b * 128 + half * 64 + l] = o;
  }
}

__global__ void __launch_bounds__(512) k_A0(const unsigned* __restrict__ x, char* ws) {
  __shared__ __attribute__((aligned(16))) unsigned lds[2560];
  int t = threadIdx.x, l = t & 63, w = t >> 6;
  int p = pidx_of_block(blockIdx.x), s2 = s2_of_pidx(p);
  const unsigned* row = x + (size_t)(s2 + 256 * w) * 256 + (l & 15) + 64 * (l >> 4);
  f32x4 re, im;
#pragma unroll
  for (int r = 0; r < 4; ++r) { unsigned u = row[16 * r]; re[r] = bflo(u); im[r] = bfhi(u); }
  if (blockIdx.x == 0) ((u32x4*)(ws + OFF_KV + 131072))[t] = u32x4{0x3f803f80u, 0x3f803f80u, 0x3f803f80u, 0x3f803f80u};
  phaseA(re, im, s2, lds, (u32x4*)(ws + OFF_U) + (size_t)p * 512, t);
}

__global__ void __launch_bounds__(512) k_B(char* ws) {
  __shared__ __attribute__((aligned(16))) unsigned lds[2 * 8 * 260];
  int t = threadIdx.x, l = t & 63, w = t >> 6;
  int cb = pidx_of_block(blockIdx.x);
  u32x4 u = ((const u32x4*)(ws + OFF_U))[(size_t)(t >> 1) * 512 + cb * 2 + (t & 1)];
  const u32x4* wt = (const u32x4*)(ws + OFF_WT) + (size_t)cb * 1024;
  u32x4 WA = wt[t], WB = wt[512 + t];
  TileInfo ti = tile_info(cb, __builtin_amdgcn_readfirstlane(w));
  Dft F = make_dft(l, -1.f, 0), I = make_dft(l, 1.f, 0), P = make_dft(l, 1.f, ti.shift);
  Tw TF = make_tw(l, -1.f, 0), TI = TF, TP = make_tw(l, 1.f, ti.shift); TI.s = -TF.s;
  float th = (float)ti.g * (1.0f / 512.0f), sn = sinr(th), cs = cosr(th);
  f32x4 ar, ai, br, bi;
#pragma unroll
  for (int r = 0; r < 4; ++r) {
    float a_r = bflo(WA[r]), a_i = bfhi(WA[r]), b_r = bflo(WB[r]), b_i = bfhi(WB[r]);
    float sr = 0.5f * (a_r + b_r), si = 0.5f * (a_i + b_i), dr = 0.5f * (a_r - b_r), di = 0.5f * (a_i - b_i);
    ar[r] = sr - sn * dr; ai[r] = si - sn * di;
    br[r] = -cs * di; bi[r] = cs * dr;
  }
#pragma unroll
  for (int i = 0; i < 4; ++i) lds[((t & 1) * 4 + i) * 260 + (t >> 1)] = u[i];
  __syncthreads();
  u32x4 own = *(const u32x4*)(lds + w * 260 + l * 4), par = *(const u32x4*)(lds + ti.partner * 260 + l * 4);
  f32x4 Ar = {0, 0, 0, 0}, Ai = Ar, Br = Ar, Bi = Ar;
  fft256(own, F, TF, F, Ar, Ai);
  fft256(par, P, TP, I, Br, Bi);
  f32x4 yr = ar * Ar - ai * Ai + br * Br + bi * Bi;
  f32x4 yi = ar * Ai + ai * Ar + bi * Br - br * Bi;
  f32x4 or_ = {0, 0, 0, 0}, oi = or_;
  fft256(pack4(yr, yi), I, TI, I, or_, oi);
  unsigned* l2 = lds + 8 * 260;
  *(u32x4*)(l2 + w * 260 + l * 4) = pack4(or_, oi);
  __syncthreads();
  u32x4 o;
#pragma unroll
  for (int i = 0; i < 4; ++i) o[i] = l2[((t & 1) * 4 + i) * 260 + (t >> 1)];
  ((u32x4*)(ws + OFF_UP))[(size_t)cb * 512 + t] = o;
}

DEVI u32x8 ldtile(const char* base, int tile, int l) {
  const u32x4* p = (const u32x4*)base + (size_t)tile * 128 + l;
  u32x4 a = p[0], b = p[64];
  return __builtin_shufflevector(a, b, 0, 1, 2, 3, 4, 5, 6, 7);
}

__global__ void __launch_bounds__(512) k_C(const unsigned* __restrict__ xin, char* ws) {
  __shared__ __attribute__((aligned(16))) unsigned lds[8 * 264 + 2048];
  int t = threadIdx.x, l = t & 63, w = __builtin_amdgcn_readfirstlane(t >> 6), c16 = l & 15, q = l >> 4;
  int p = pidx_of_block(blockIdx.x), s2 = s2_of_pidx(p);
  u32x4 u = ((const u32x4*)(ws + OFF_UP))[(size_t)(t >> 1) * 512 + p * 2 + (t & 1)];
  size_t rowoff = (size_t)(s2 + 256 * w) * 256 + c16 + 64 * q;
  f32x4 re, im;
#pragma unroll
  for (int r = 0; r < 4; ++r) { unsigned v = xin[rowoff + 16 * r]; re[r] = bflo(v); im[r] = bfhi(v); }
  Dft I = make_dft(l, 1.f, 0); Tw TI = make_tw(l, 1.f, 0);
  u32x2 m1, m2; make_r8<true>(l, s2, m1, m2);
  f32x4 z = {0, 0, 0, 0};
  f32x4 e1 = mf16(m1, u32x2{u[0], u[1]}, z), e2 = mf16(m2, u32x2{u[2], u[3]}, z);
  int sg = 16 * w + c16, g2 = sg ? 256 - sg : 128;
  lds[(2 * q) * 264 + sg] = pack2(e1[0], e1[1]); lds[(2 * q + 1) * 264 + sg] = pack2(e1[2], e1[3]);
  lds[(2 * q) * 264 + g2] = pack2(e2[0], e2[1]); lds[(2 * q + 1) * 264 + g2] = pack2(e2[2], e2[3]);
  __syncthreads();
  u32x4 a;
#pragma unroll
  for (int r = 0; r < 4; ++r) a[r] = lds[w * 264 + c16 + 16 * (4 * q + r)];
  fft256(a, I, TI, I, re, im);
  u32x4 xb = pack4(re, im);
  unsigned* x1 = (unsigned*)(ws + OFF_X1);
#pragma unroll
  for (int r = 0; r < 4; ++r) { x1[rowoff + 16 * r] = xb[r]; re[r] = bflo(xb[r]); im[r] = bfhi(xb[r]); }
  u32x4 ln = layernorm_pack(re, im);
  u32x4* lf = (u32x4*)(lds + 8 * 264);
  { int ks = l >> 3, ga = (l >> 1) & 3, pp = l & 1; int idx = ks * 64 + ga * 16 + ((2 * w + pp) ^ (2 * ga));
    lf[idx] = ln; ((u32x4*)(ws + OFF_LN))[(size_t)p * 512 + idx] = ln; }
  __syncthreads();
  if (w < 2) {
    int sidx = (l & 1) ? 0xeeee : 0x4444;
    f32x4 acc = z;
#pragma unroll
    for (int ks = 0; ks < 8; ++ks) acc = smf64(lf[ks * 64 + q * 16 + (c16 ^ (2 * q))], ldtile(ws + OFF_WKV, w * 8 + ks, l), acc, sidx);
    float a0 = acc[0] + acc[1], a1 = acc[2] + acc[3];
    unsigned* kv = (unsigned*)(ws + OFF_KV);
    if (w == 0) {
      bool odd = l & 1;
      float snd = odd ? a0 : a1, keep = odd ? a1 : a0;
      float rcv = __builtin_bit_cast(float, __builtin_amdgcn_update_dpp(0, __builtin_bit_cast(int, snd), 0xB1, 0xf, 0xf, true));
      unsigned pk = odd ? pack2(rcv, keep) : pack2(keep, rcv);
      int tok = 2 * q + (l & 1), h = c16 >> 3;
      kv[(p >> 2) * 256 + (h * 32 + (p & 3) * 8 + tok) * 4 + ((c16 >> 1) & 3)] = pk;
    } else {
      kv[16384 + p * 64 + c16 * 4 + q] = pack2(a0, a1);
    }
  }
}

DEVI bf16x16 lo8(f32x16 s) { return __builtin_bit_cast(bf16x16, __builtin_shufflevector(s, s, 0, 1, 2, 3, 4, 5, 6, 7)); }
DEVI bf16x16 hi8(f32x16 s) { return __builtin_bit_cast(bf16x16, __builtin_shufflevector(s, s, 8, 9, 10, 11, 12, 13, 14, 15)); }
#define QKM(k, q, c) __builtin_amdgcn_mfma_f32_32x32x16_bf16(__builtin_bit_cast(bf16x8, k), __builtin_bit_cast(bf16x8, q), c, 0, 0, 0)
#define PVM(v, p, o) __builtin_amdgcn_smfmac_f32_32x32x32_bf16(__builtin_bit_cast(bf16x8, v), p, o, 0xdddd, 0, 0)

template <bool LAST>
__global__ void __launch_bounds__(512, 2) k_D(char* ws, unsigned* __restrict__ out) {
  __shared__ __attribute__((aligned(16))) unsigned lds[2048 + 2048 + 4608 + 256 + 8 * 544];
  u32x4* lf = (u32x4*)lds; u16* qimg = (u16*)(lds + 2048); float* part = (float*)(lds + 4096);
  u16* ofr = (u16*)(lds + 4096 + 4608); float* rows = (float*)(lds + 4096 + 4608 + 256);
  int t = threadIdx.x, l = t & 63, w = __builtin_amdgcn_readfirstlane(t >> 6), c16 = l & 15, q = l >> 4;
  int p = pidx_of_block(blockIdx.x), s2 = s2_of_pidx(p);
  const __amdgpu_buffer_rsrc_t kvr = __builtin_amdgcn_make_buffer_rsrc(ws + OFF_KV, 0, KV_BYTES, 0x00020000);
  u32x4 kf[8], vf[16];
  { int d = l & 31, h = l >> 5;
    unsigned ko = w * 8192 + l * 16;
    unsigned vo = d < 16 ? 65536u + w * 8192 + h * 256 + d * 16 : (d == 16 ? 131072u : 0x7f000000u);
#pragma unroll
    for (int i = 0; i < 8; ++i) kf[i] = __builtin_amdgcn_raw_buffer_load_b128(kvr, ko + i * 1024, 0, 0);
#pragma unroll
    for (int i = 0; i < 16; ++i) vf[i] = __builtin_amdgcn_raw_buffer_load_b128(kvr, vo + i * 512, 0, 0);
  }
  lf[t] = ((const u32x4*)(ws + OFF_LN))[(size_t)p * 512 + t];
  size_t rowoff = (size_t)(s2 + 256 * w) * 256 + c16 + 64 * q;
  unsigned* x1 = (unsigned*)(ws + OFF_X1);
  u32x4 xb;
#pragma unroll
  for (int r = 0; r < 4; ++r) xb[r] = x1[rowoff + 16 * r];
  __syncthreads();
  int sidx = (l & 1) ? 0xeeee : 0x4444;
  f32x4 z4 = {0, 0, 0, 0};
#pragma unroll 1
  for (int sup = 0; sup < 4; ++sup) {
    f32x4 acc = z4;
#pragma unroll
    for (int ks = 0; ks < 8; ++ks) acc = smf64(lf[ks * 64 + q * 16 + (c16 ^ (2 * q))], ldtile(ws + OFF_WQ, (sup * 8 + w) * 8 + ks, l), acc, sidx);
    const float SC = 0.25f * 1.44269504089f;
    unsigned pk = pack2((acc[0] + acc[1]) * SC, (acc[2] + acc[3]) * SC);
    int qt = 2 * sup + (w >> 2), qv = (w & 1) + 4 * q + 16 * ((w >> 1) & 1);
    int e = qt * 512 + (qv + 32 * (c16 >> 3)) * 8 + (c16 & 7);
    qimg[e] = (u16)pk; qimg[e + 16] = (u16)(pk >> 16);
  }
  __syncthreads();
  f32x4 oacc[4] = {z4, z4, z4, z4};
#pragma unroll 1
  for (int s = 0; s < 8; ++s) {
    u32x4 qf = ((const u32x4*)qimg)[s * 64 + l];
    u32x8 wt[4];
#pragma unroll
    for (int i = 0; i < 4; ++i) wt[i] = ldtile(ws + OFF_WO, (s * 8 + w) * 4 + i, l);
    f32x16 z = {0}, o = {0};
#pragma unroll
    for (int i = 0; i < 8; ++i) {
      f32x16 sc = QKM(kf[i], qf, z);
#pragma unroll
      for (int j = 0; j < 16; ++j) sc[j] = __builtin_amdgcn_exp2f(sc[j]);
      o = PVM(vf[2 * i], lo8(sc), o);
      o = PVM(vf[2 * i + 1], hi8(sc), o);
    }
#pragma unroll
    for (int r = 0; r < 9; ++r) part[(r * 8 + w) * 64 + l] = o[r];
    __syncthreads();
    float so = 0, sl = 0;
#pragma unroll
    for (int v = 0; v < 8; ++v) { so += part[(w * 8 + v) * 64 + l]; sl += part[(64 + v) * 64 + (l & 31)]; }
    ofr[l * 8 + w] = (u16)pack2(so * __builtin_amdgcn_rcpf(sl), 0.f);
    __syncthreads();
    u32x4 of = ((const u32x4*)ofr)[l];
#pragma unroll
    for (int i = 0; i < 4; ++i) oacc[i] = smf64(of, wt[i], oacc[i], sidx);
  }
  {
    f32x4 t0 = {oacc[0][0] + oacc[0][1], oacc[1][0] + oacc[1][1], oacc[2][0] + oacc[2][1], oacc[3][0] + oacc[3][1]};
    f32x4 t1 = {oacc[0][2] + oacc[0][3], oacc[1][2] + oacc[1][3], oacc[2][2] + oacc[2][3], oacc[3][2] + oacc[3][3]};
    int off = (c16 & 1) * 272 + (w * 8 + (c16 >> 1)) * 4;
    *(f32x4*)(rows + (2 * q) * 544 + off) = t0;
    *(f32x4*)(rows + (2 * q + 1) * 544 + off) = t1;
  }
  __syncthreads();
  f32x4 re = *(const f32x4*)(rows + w * 544 + l * 4), im = *(const f32x4*)(rows + w * 544 + 272 + l * 4);
#pragma unroll
  for (int r = 0; r < 4; ++r) { re[r] += bflo(xb[r]); im[r] += bfhi(xb[r]); }
  xb = pack4(re, im);
  unsigned* dst = LAST ? out : x1;
#pragma unroll
  for (int r = 0; r < 4; ++r) { dst[rowoff + 16 * r] = xb[r]; re[r] = bflo(xb[r]); im[r] = bfhi(xb[r]); }
  if (!LAST) phaseA(re, im, s2, lds, (u32x4*)(ws + OFF_U) + (size_t)p * 512, t);
}

extern "C" void kernel_launch(void* const* d_in, const int* in_sizes, int n_in, void* d_out, int out_size, void* d_ws,
                              size_t ws_size, hipStream_t stream) {
  const unsigned* x = (const unsigned*)d_in[0];
  char* ws = (char*)d_ws;
  k_prep<<<256, 512, 0, stream>>>((const u16*)d_in[1], (const u16*)d_in[2], (const u16*)d_in[4], (const u16*)d_in[5],
                                  (const u16*)d_in[6], (const u16*)d_in[7], ws);
  k_A0<<<256, 512, 0, stream>>>(x, ws);
  k_B<<<256, 512, 0, stream>>>(ws);
  k_C<<<256, 512, 0, stream>>>(x, ws);
  k_D<false><<<256, 512, 0, stream>>>(ws, (unsigned*)d_out);
  k_B<<<256, 512, 0, stream>>>(ws);
  k_C<<<256, 512, 0, stream>>>((const unsigned*)(ws + OFF_X1), ws);
  k_D<true><<<256, 512, 0, stream>>>(ws, (unsigned*)d_out);
}
```

```cpp
#include <hip/hip_runtime.h>
#include <stdint.h>

typedef float f32x2 __attribute__((ext_vector_type(2)));
typedef float f32x4 __attribute__((ext_vector_type(4)));
typedef float f32x8 __attribute__((ext_vector_type(8)));
typedef float f32x16 __attribute__((ext_vector_type(16)));
typedef __bf16 bf16x2 __attribute__((ext_vector_type(2)));
typedef __bf16 bf16x8 __attribute__((ext_vector_type(8)));
typedef __bf16 bf16x16 __attribute__((ext_vector_type(16)));
typedef short s16x4 __attribute__((ext_vector_type(4)));
typedef unsigned u32x2 __attribute__((ext_vector_type(2)));
typedef unsigned u32x4 __attribute__((ext_vector_type(4)));
typedef unsigned u32x8 __attribute__((ext_vector_type(8)));
typedef unsigned short u16;
typedef __amdgpu_buffer_rsrc_t rsrc_t;

#define DEVI __device__ __forceinline__
#define SB() __builtin_amdgcn_sched_barrier(0)
#define BAR() do { asm volatile("s_waitcnt lgkmcnt(0)\n\ts_barrier" ::: "memory"); SB(); } while (0)
#define PIN(x) asm volatile("" : "+v"(x))
#define UNDEF(x) asm volatile("" : "=v"(x))
#define NEW_PAGE() asm volatile("s_branch 1f\n\t.p2align 12\n1:" ::: "memory")
#define DRAIN() asm volatile("s_waitcnt vmcnt(0)" ::: "memory")
constexpr int SC1 = 16;

constexpr unsigned MB = 1u << 20;
constexpr unsigned OFF_U   = 0;
constexpr unsigned OFF_UP  = 4 * MB;
constexpr unsigned OFF_WR  = 8 * MB;
constexpr unsigned OFF_ST  = 10 * MB;
constexpr unsigned OFF_WKV = 11 * MB;
constexpr unsigned OFF_KV  = OFF_WKV + 32768;
constexpr unsigned KV_STRIDE = 131072;
constexpr unsigned OFF_WS  = OFF_KV + 2 * KV_STRIDE;
constexpr unsigned OFF_FLG = OFF_WS + 32768;
constexpr unsigned OFF_EPO = OFF_FLG + 3 * 32768;
constexpr unsigned WS_USED = OFF_EPO + 256;
constexpr unsigned OFF_STAMP = 16 * MB;
constexpr unsigned SENT = 0xFFFFFFFFu;
constexpr unsigned SPIN_CAP = 1u << 16;

#define CODE_TOUCH 42880
#define CODE_OFF (t * 64)
#define STAMP_PARAMS
#define STAMP_ARGS
#define WS_BASE 0
#define ATT_LAYER 1
#define Q1PRE
#define DLY_E 5
#define OUTV 0
#define PFV 0
#define UP_LATE 0
#define FLAG_STRIDE 32
#define DLY_C0 0
#define DLY_F 0
#define DLY_B 7
#define DLY_C 8
#define DLY_D 10
#define RETRY_SLEEP 2
constexpr float GAM = 145.0f / 512.0f;
constexpr float TWS = (128.0f / 145.0f) * (128.0f / 145.0f);

#define STAMP(i)

DEVI unsigned pack2(float a, float b) { bf16x2 v = {(__bf16)a, (__bf16)b}; return __builtin_bit_cast(unsigned, v); }
DEVI float bflo(unsigned u) { return __builtin_bit_cast(float, u << 16); }
DEVI float bfhi(unsigned u) { return __builtin_bit_cast(float, u & 0xffff0000u); }
DEVI float sinr(float rev) { return __builtin_amdgcn_sinf(rev); }
DEVI float cosr(float rev) { return __builtin_amdgcn_cosf(rev); }
DEVI f32x4 mf32(u32x4 a, u32x4 b, f32x4 c) {
  return __builtin_amdgcn_mfma_f32_16x16x32_bf16(__builtin_bit_cast(bf16x8, a), __builtin_bit_cast(bf16x8, b), c, 0, 0, 0);
}
DEVI f32x4 mf16(u32x2 a, u32x2 b, f32x4 c) {
  return __builtin_amdgcn_mfma_f32_16x16x16bf16_1k(__builtin_bit_cast(s16x4, a), __builtin_bit_cast(s16x4, b), c, 0, 0, 0);
}
DEVI f32x4 smf64(u32x4 a, u32x8 b, f32x4 c, int idx) {
  return __builtin_amdgcn_smfmac_f32_16x16x64_bf16(__builtin_bit_cast(bf16x8, a), __builtin_bit_cast(bf16x16, b), c, idx, 0, 0);
}
DEVI u32x4 pack4(f32x4 re, f32x4 im) { return u32x4{pack2(re[0], im[0]), pack2(re[1], im[1]), pack2(re[2], im[2]), pack2(re[3], im[3])}; }
DEVI u32x4 ld128(rsrc_t rs, unsigned voff, unsigned soff, int aux_sc1) {
  return aux_sc1 ? __builtin_amdgcn_raw_buffer_load_b128(rs, voff, soff, SC1) : __builtin_amdgcn_raw_buffer_load_b128(rs, voff, soff, 0);
}
DEVI void st128(u32x4 v, rsrc_t rs, unsigned voff, unsigned soff) {
  __builtin_amdgcn_raw_buffer_store_b128(v, rs, voff, soff, SC1);
  asm volatile("s_nop 1" :: "v"(v));
}
template <class T> DEVI T sel(bool c, T a, T b) { return c ? a : b; }
DEVI unsigned max3u(unsigned a, unsigned b, unsigned c) { unsigned m = a > b ? a : b; return m > c ? m : c; }

template <int CTRL> DEVI float dppadd(float x) {
  return x + __builtin_bit_cast(float, __builtin_amdgcn_update_dpp(0, __builtin_bit_cast(int, x), CTRL, 0xf, 0xf, true));
}
DEVI float rdlane(float x, int i) { return __builtin_bit_cast(float, __builtin_amdgcn_readlane(__builtin_bit_cast(int, x), i)); }
DEVI void wave_sum2(float a, float b, float& sa, float& sb) {
  auto r = __builtin_amdgcn_permlane32_swap(__builtin_bit_cast(unsigned, a), __builtin_bit_cast(unsigned, b), false, false);
  const unsigned r0 = r[0], r1 = r[1];
  float x = __builtin_bit_cast(float, r0) + __builtin_bit_cast(float, r1);
  x = dppadd<0xB1>(x); x = dppadd<0x4E>(x); x = dppadd<0x141>(x); x = dppadd<0x140>(x);
  x = dppadd<0x142>(x);
  sa = rdlane(x, 31); sb = rdlane(x, 63);
}

struct Dft { u32x4 re, im; };
struct Tw { f32x4 c, s; };
DEVI Dft make_dft(int l, float sgn, int shift) {
  Dft d; int k = (l & 15) + shift, q = l >> 4;
#pragma unroll
  for (int r = 0; r < 4; ++r) {
    float rev = (float)(((4 * q + r) * k) & 15) * (1.0f / 16.0f);
    float c = GAM * cosr(rev), s = sgn * GAM * sinr(rev);
    d.re[r] = pack2(c, -s); d.im[r] = pack2(s, c);
  }
  return d;
}
DEVI Tw make_tw(int l, float sgn, int shift) {
  Tw t; int k = (l & 15) + shift, q = l >> 4;
#pragma unroll
  for (int r = 0; r < 4; ++r) {
    float rev = (float)((4 * q + r) * k) * (1.0f / 256.0f);
    t.c[r] = TWS * cosr(rev); t.s[r] = sgn * TWS * sinr(rev);
  }
  return t;
}
DEVI void fft256(u32x4 a, const Dft& d1, const Tw& t, const Dft& d2, f32x4& ore, f32x4& oim) {
  f32x4 z = {0, 0, 0, 0};
  f32x4 re = mf32(a, d1.re, z), im = mf32(a, d1.im, z);
  f32x4 r2 = re * t.c - im * t.s, i2 = re * t.s + im * t.c;
  u32x4 p = pack4(r2, i2);
  ore = mf32(d2.re, p, ore); oim = mf32(d2.im, p, oim);
}
DEVI int kappa(int idx) { return __builtin_amdgcn_sbfe(0x40D3E2F1, 4 * idx, 4); }
DEVI int pidx_of_block(int b) { return (b & 7) * 32 + (b >> 3); }
DEVI int s2_of_pidx(int p) { int r = p & 3, lb = p >> 2; return 16 * (4 * (lb >> 4) + r) + (lb & 15); }
DEVI int pidx_of_s2(int k) { return (k & 192) + ((k & 15) << 2) + ((k >> 4) & 3); }
template <bool INV> DEVI void make_r8(int l, int s2, u32x2& m1, u32x2& m2) {
  int row = l & 15, q = l >> 4, co = row & 1;
#pragma unroll
  for (int e = 0; e < 2; ++e) {
    int a = row >> 1, b = 2 * q + e;
    int idx = INV ? b : a, j = INV ? a : b;
    int n = ((s2 + 256 * j) * kappa(idx)) & 2047;
    float rev = (float)n * (1.0f / 2048.0f);
    float c = 0.35355339059f * cosr(rev), s = (INV ? 0.35355339059f : -0.35355339059f) * sinr(rev);
    m1[e] = pack2(co ? s : c, co ? c : -s);
    m2[e] = pack2(co ? -s : c, co ? c : s);
  }
}
DEVI u32x4 layernorm_pack(f32x4 re, f32x4 im) {
  const f32x4 s4 = re + im, q4 = re * re + im * im;
  const f32x2 s2_ = f32x2{s4[0], s4[1]} + f32x2{s4[2], s4[3]}, q2 = f32x2{q4[0], q4[1]} + f32x2{q4[2], q4[3]};
  float s, q;
  wave_sum2(s2_[0] + s2_[1], q2[0] + q2[1], s, q);
  const float m = s * (1.0f / 512.0f);
  const float rs = __builtin_amdgcn_rsqf(__builtin_fmaxf(__builtin_fmaf(-m, m, __builtin_fmaf(q, 1.0f / 512.0f, 1e-5f)), 1e-5f)), o = -m * rs;
  return pack4(re * rs + o, im * rs + o);
}

struct Slot { int kind, a, b; };
__host__ __device__ constexpr Slot slot_of(int n) {
  if (n < 32) return {0, n >> 3, n & 7};
  if (n < 96) return ((n - 32) & 8) ? Slot{0, 4 + ((n - 32) >> 4), n & 7} : Slot{1, (n - 32) >> 4, n & 7};
  return {1, 4 + ((n - 96) >> 3), n & 7};
}
__host__ __device__ constexpr int out_slot0(int s) { return s < 4 ? 32 + 16 * s : 64 + 8 * s; }
__host__ __device__ constexpr int q_slot0(int s) { return s < 4 ? 8 * s : 16 * s - 24; }

DEVI bf16x16 lo8(f32x16 s) { return __builtin_bit_cast(bf16x16, __builtin_shufflevector(s, s, 0, 1, 2, 3, 4, 5, 6, 7)); }
DEVI bf16x16 hi8(f32x16 s) { return __builtin_bit_cast(bf16x16, __builtin_shufflevector(s, s, 8, 9, 10, 11, 12, 13, 14, 15)); }
#define QKM(k, q, c) __builtin_amdgcn_mfma_f32_32x32x16_bf16(__builtin_bit_cast(bf16x8, k), __builtin_bit_cast(bf16x8, q), c, 0, 0, 0)
#define PVM(v, p, o) __builtin_amdgcn_smfmac_f32_32x32x32_bf16(__builtin_bit_cast(bf16x8, v), p, o, 0xdddd, 0, 0)

constexpr int L_LF = 0, L_QI = 2048, L_PART = 4096, L_R1 = L_PART + 6144, L_OFR = L_R1 + 4608, L_XS = L_OFR + 512, L_CT = L_XS + 6144,
              L_FLAG = L_CT + 1280, L_WST = L_FLAG + 4, L_WKV = L_WST + 16 * 256, L_END = L_WKV + 8192;

DEVI u32x4 mk_sent() { unsigned x; asm volatile("v_mov_b32 %0, -1" : "=v"(x)); return u32x4{x, x, x, x}; }
DEVI f32x4 mk_zero4() { f32x2 x, y; asm volatile("v_mov_b64 %0, 0\n\tv_mov_b64 %1, 0\n\ts_nop 1" : "=v"(x), "=v"(y)); return f32x4{x[0], x[1], y[0], y[1]}; }

DEVI bool has_sent(u32x4 v) { return max3u(max3u(v[0], v[1], v[2]), v[3], 0) == SENT; }
DEVI u32x4 poll128(rsrc_t rs, unsigned voff, unsigned soff, u32x4 v) {
  if (__builtin_expect(__any(has_sent(v)), 0)) {
#pragma unroll 1
    for (unsigned spins = 0; spins < SPIN_CAP; ++spins) {
      asm volatile("" ::: "memory");
      if (has_sent(v)) v = ld128(rs, voff, soff, 1);
      if (!__any(has_sent(v))) break;
      __builtin_amdgcn_s_sleep(RETRY_SLEEP);
    }
  }
  return v;
}
DEVI u32x8 ldtile(rsrc_t rs, unsigned voff, unsigned soff) {
  u32x4 a = ld128(rs, voff, soff, 1), b = ld128(rs, voff + 1024, soff, 1);
  return __builtin_shufflevector(a, b, 0, 1, 2, 3, 4, 5, 6, 7);
}
DEVI unsigned ld16(rsrc_t rs, unsigned elem) { return __builtin_amdgcn_raw_buffer_load_b16(rs, elem * 2, 0, 0); }
DEVI u32x4 ldLF(const unsigned* lds, int l, int ks) { int q = l >> 4; return ((const u32x4*)(lds + L_LF))[ks * 64 + q * 16 + ((l & 15) ^ (2 * q))]; }

struct Unit { int g, t1, t2, shift, on, wa; };
DEVI Unit unit_of(int qk, int sg0, int u) {
  Unit n; const int ks = u & 1, sg = sg0 + ((u >> 1) & 1);
  const bool z = sg == 0, x4 = z & (qk == 3) & (u == 4);
  n.g = sel(z, ks * 128, sg);
  n.t1 = sel(z, sel(x4, 1, 2 * ks), (u >> 1) * 4 + ks);
  n.t2 = n.t1 + sel(z, sel((qk == 3) & (ks == 0), 0, 1), 2);
  n.shift = x4;
  n.on = (u < 4) | x4;
  n.wa = sel(z, sel(x4, 2, 12 * ks), 2 * u);
  return n;
}
typedef __attribute__((address_space(3))) char* lptr;
template <class T> DEVI T ldsr(unsigned a) { return *(__attribute__((address_space(3))) const T*)(lptr)(size_t)a; }
template <class T> DEVI void ldsw(unsigned a, T v) { *(__attribute__((address_space(3))) T*)(lptr)(size_t)a = v; }
DEVI u32x2 tr_read(const unsigned* p) {
  return __builtin_bit_cast(u32x2, __builtin_amdgcn_ds_read_tr16_b64_v4i16((__attribute__((address_space(3))) s16x4*)p));
}
DEVI u32x2 flag_load(rsrc_t rs, int t, int k) { return __builtin_amdgcn_raw_buffer_load_b64(rs, t * FLAG_STRIDE, OFF_FLG + k * 32768, SC1); }
DEVI void flag_wait(rsrc_t rs, int t, int k, u32x2 f, unsigned lo, unsigned hi) {
  if (__builtin_expect(__any(((f[0] ^ lo) | (f[1] ^ hi)) != 0), 0)) {
#pragma unroll 1
    for (unsigned spins = 0; spins < SPIN_CAP; ++spins) {
      asm volatile("" ::: "memory");
      if (((f[0] ^ lo) | (f[1] ^ hi)) != 0) f = flag_load(rs, t, k);
      if (!__any(((f[0] ^ lo) | (f[1] ^ hi)) != 0)) break;
      __builtin_amdgcn_s_sleep(RETRY_SLEEP);
    }
  }
  asm volatile("" ::: "memory");
}
DEVI unsigned ld16o(rsrc_t rs, unsigned voff, unsigned soff) { return __builtin_amdgcn_raw_buffer_load_b16(rs, voff, soff, 0); }

#define ATT_ASM \
  "s_nop 4\n" \
  "s_cmp_lt_u32 %[w], 4\n" \
  "s_cbranch_scc1 .LA_%=\n" \
  "s_branch .LB_%=\n" \
  ".p2align 12\n" \
  ".LB_%=:\n" \
  "ds_read_b128 v[8:11], v1 offset:0\n" \
  "ds_read_b128 v[12:15], v1 offset:1024\n" \
  "ds_read_b128 v[16:19], v1 offset:2048\n" \
  "ds_read_b128 v[20:23], v1 offset:3072\n" \
  "ds_read_b128 v[24:27], v1 offset:4096\n" \
  "ds_read_b128 v[28:31], v1 offset:5120\n" \
  "ds_read_b128 v[32:35], v1 offset:6144\n" \
  "ds_read_b128 v[36:39], v1 offset:7168\n" \
  "s_mov_b64 %[bad], 0\n" \
  "s_cmp_eq_u32 %[po1], 0\n" \
  "s_cbranch_scc1 .Lq1_%=\n" \
  "buffer_load_dwordx4 v[248:251], v0, %[rs], %[stb] offen offset:2048 sc1\n" \
  "buffer_load_dwordx4 v[252:255], v0, %[rs], %[stb] offen offset:3072 sc1\n" \
  "buffer_load_dwordx4 v[240:243], v0, %[rs], %[stb] offen offset:0 sc1\n" \
  "buffer_load_dwordx4 v[244:247], v0, %[rs], %[stb] offen offset:1024 sc1\n" \
  "s_sub_u32 %[stb], %[stb], 0x1000\n" \
  "buffer_load_dwordx4 v[232:235], v0, %[rs], %[stb] offen offset:2048 sc1\n" \
  "buffer_load_dwordx4 v[236:239], v0, %[rs], %[stb] offen offset:3072 sc1\n" \
  "buffer_load_dwordx4 v[224:227], v0, %[rs], %[stb] offen offset:0 sc1\n" \
  "buffer_load_dwordx4 v[228:231], v0, %[rs], %[stb] offen offset:1024 sc1\n" \
  "s_sub_u32 %[stb], %[stb], 0x1000\n" \
  "buffer_load_dwordx4 v[216:219], v0, %[rs], %[stb] offen offset:2048 sc1\n" \
  "buffer_load_dwordx4 v[220:223], v0, %[rs], %[stb] offen offset:3072 sc1\n" \
  "buffer_load_dwordx4 v[208:211], v0, %[rs], %[stb] offen offset:0 sc1\n" \
  "buffer_load_dwordx4 v[212:215], v0, %[rs], %[stb] offen offset:1024 sc1\n" \
  "s_sub_u32 %[stb], %[stb], 0x1000\n" \
  "buffer_load_dwordx4 v[200:203], v0, %[rs], %[stb] offen offset:2048 sc1\n" \
  "buffer_load_dwordx4 v[204:207], v0, %[rs], %[stb] offen offset:3072 sc1\n" \
  "buffer_load_dwordx4 v[192:195], v0, %[rs], %[stb] offen offset:0 sc1\n" \
  "buffer_load_dwordx4 v[196:199], v0, %[rs], %[stb] offen offset:1024 sc1\n" \
  "s_add_u32 %[stb], %[stb], 0x3000\n" \
  ".Lq1_%=:\n" \
  "v_mov_b64 v[72:73], 0\n" \
  "v_mov_b64 v[74:75], 0\n" \
  "s_nop 1\n" \
  "s_waitcnt lgkmcnt(7)\n" \
  "v_smfmac_f32_16x16x64_bf16 v[72:75], v[8:11], v[128:135], v7\n" \
  "s_waitcnt lgkmcnt(6)\n" \
  "v_smfmac_f32_16x16x64_bf16 v[72:75], v[12:15], v[136:143], v7\n" \
  "s_waitcnt lgkmcnt(5)\n" \
  "v_smfmac_f32_16x16x64_bf16 v[72:75], v[16:19], v[144:151], v7\n" \
  "s_waitcnt lgkmcnt(4)\n" \
  "v_smfmac_f32_16x16x64_bf16 v[72:75], v[20:23], v[152:159], v7\n" \
  "s_waitcnt lgkmcnt(3)\n" \
  "v_smfmac_f32_16x16x64_bf16 v[72:75], v[24:27], v[160:167], v7\n" \
  "s_waitcnt lgkmcnt(2)\n" \
  "v_smfmac_f32_16x16x64_bf16 v[72:75], v[28:31], v[168:175], v7\n" \
  "s_waitcnt lgkmcnt(1)\n" \
  "v_smfmac_f32_16x16x64_bf16 v[72:75], v[32:35], v[176:183], v7\n" \
  "s_waitcnt lgkmcnt(0)\n" \
  "v_smfmac_f32_16x16x64_bf16 v[72:75], v[36:39], v[184:191], v7\n" \
  "s_nop 7\n" \
  "v_add_f32 v84, v72, v73\n" \
  "v_add_f32 v85, v74, v75\n" \
  "v_pk_mul_f32 v[84:85], v[84:85], %[sc]\n" \
  "v_cvt_pk_bf16_f32 v84, v84, v85\n" \
  "ds_write_b16 v2, v84 offset:8192\n" \
  "ds_write_b16_d16_hi v2, v84 offset:8224\n" \
  "s_waitcnt lgkmcnt(0)\n" \
  "s_barrier\n" \
  "s_setprio 2\n" \
  "v_mov_b64 v[72:73], 0\n" \
  "v_mov_b64 v[74:75], 0\n" \
  "s_nop 1\n" \
  "s_waitcnt vmcnt(0)\n" \
  "v_smfmac_f32_16x16x64_bf16 v[72:75], v[8:11], v[192:199], v7\n" \
  "v_smfmac_f32_16x16x64_bf16 v[72:75], v[12:15], v[200:207], v7\n" \
  "v_smfmac_f32_16x16x64_bf16 v[72:75], v[16:19], v[208:215], v7\n" \
  "v_smfmac_f32_16x16x64_bf16 v[72:75], v[20:23], v[216:223], v7\n" \
  "v_smfmac_f32_16x16x64_bf16 v[72:75], v[24:27], v[224:231], v7\n" \
  "v_smfmac_f32_16x16x64_bf16 v[72:75], v[28:31], v[232:239], v7\n" \
  "v_smfmac_f32_16x16x64_bf16 v[72:75], v[32:35], v[240:247], v7\n" \
  "v_smfmac_f32_16x16x64_bf16 v[72:75], v[36:39], v[248:255], v7\n" \
  "s_nop 7\n" \
  "v_add_f32 v84, v72, v73\n" \
  "v_add_f32 v85, v74, v75\n" \
  "v_pk_mul_f32 v[84:85], v[84:85], %[sc]\n" \
  "v_cvt_pk_bf16_f32 v84, v84, v85\n" \
  "ds_write_b16 v2, v84 offset:9216\n" \
  "ds_write_b16_d16_hi v2, v84 offset:9248\n" \
  "v_mov_b64 v[40:41], 0\n" \
  "v_mov_b64 v[42:43], 0\n" \
  "v_mov_b64 v[44:45], 0\n" \
  "v_mov_b64 v[46:47], 0\n" \
  "v_mov_b64 v[48:49], 0\n" \
  "v_mov_b64 v[50:51], 0\n" \
  "v_mov_b64 v[52:53], 0\n" \
  "v_mov_b64 v[54:55], 0\n" \
  "v_mov_b64 v[56:57], 0\n" \
  "v_mov_b64 v[58:59], 0\n" \
  "v_mov_b64 v[60:61], 0\n" \
  "v_mov_b64 v[62:63], 0\n" \
  "v_mov_b64 v[64:65], 0\n" \
  "v_mov_b64 v[66:67], 0\n" \
  "v_mov_b64 v[68:69], 0\n" \
  "v_mov_b64 v[70:71], 0\n" \
  "s_mov_b64 exec, 0\n" \
  "s_branch .Lpad0_%=\n" \
  ".Lmqa_padret:\n" \
  "s_mov_b64 exec, -1\n" \
  "s_waitcnt lgkmcnt(0)\n" \
  "s_barrier\n" \
  "s_add_u32 %[stb], %[stb], 0x1000\n" \
  "buffer_load_dwordx4 v[128:131], v0, %[rs], %[stb] offen offset:0 sc1\n" \
  "buffer_load_dwordx4 v[132:135], v0, %[rs], %[stb] offen offset:1024 sc1\n" \
  "buffer_load_dwordx4 v[136:139], v0, %[rs], %[stb] offen offset:2048 sc1\n" \
  "buffer_load_dwordx4 v[140:143], v0, %[rs], %[stb] offen offset:3072 sc1\n" \
  "s_add_u32 %[stb], %[stb], 0x1000\n" \
  "buffer_load_dwordx4 v[144:147], v0, %[rs], %[stb] offen offset:0 sc1\n" \
  "buffer_load_dwordx4 v[148:151], v0, %[rs], %[stb] offen offset:1024 sc1\n" \
  "buffer_load_dwordx4 v[152:155], v0, %[rs], %[stb] offen offset:2048 sc1\n" \
  "buffer_load_dwordx4 v[156:159], v0, %[rs], %[stb] offen offset:3072 sc1\n" \
  "s_add_u32 %[stb], %[stb], 0x1000\n" \
  "buffer_load_dwordx4 v[160:163], v0, %[rs], %[stb] offen offset:0 sc1\n" \
  "buffer_load_dwordx4 v[164:167], v0, %[rs], %[stb] offen offset:1024 sc1\n" \
  "buffer_load_dwordx4 v[168:171], v0, %[rs], %[stb] offen offset:2048 sc1\n" \
  "buffer_load_dwordx4 v[172:175], v0, %[rs], %[stb] offen offset:3072 sc1\n" \
  "s_add_u32 %[stb], %[stb], 0x1000\n" \
  "buffer_load_dwordx4 v[176:179], v0, %[rs], %[stb] offen offset:0 sc1\n" \
  "buffer_load_dwordx4 v[180:183], v0, %[rs], %[stb] offen offset:1024 sc1\n" \
  "buffer_load_dwordx4 v[184:187], v0, %[rs], %[stb] offen offset:2048 sc1\n" \
  "buffer_load_dwordx4 v[188:191], v0, %[rs], %[stb] offen offset:3072 sc1\n" \
  "s_add_u32 %[stb], %[stb], 0x1000\n" \
  "buffer_load_dwordx4 v[192:195], v0, %[rs], %[stb] offen offset:0 sc1\n" \
  "buffer_load_dwordx4 v[196:199], v0, %[rs], %[stb] offen offset:1024 sc1\n" \
  "buffer_load_dwordx4 v[200:203], v0, %[rs], %[stb] offen offset:2048 sc1\n" \
  "buffer_load_dwordx4 v[204:207], v0, %[rs], %[stb] offen offset:3072 sc1\n" \
  "s_add_u32 %[stb], %[stb], 0x1000\n" \
  "buffer_load_dwordx4 v[208:211], v0, %[rs], %[stb] offen offset:0 sc1\n" \
  "buffer_load_dwordx4 v[212:215], v0, %[rs], %[stb] offen offset:1024 sc1\n" \
  "buffer_load_dwordx4 v[216:219], v0, %[rs], %[stb] offen offset:2048 sc1\n" \
  "buffer_load_dwordx4 v[220:223], v0, %[rs], %[stb] offen offset:3072 sc1\n" \
  "v_mov_b64 v[72:73], 0\n" \
  "v_mov_b64 v[74:75], 0\n" \
  "s_nop 1\n" \
  "s_waitcnt vmcnt(22)\n" \
  "v_smfmac_f32_16x16x64_bf16 v[72:75], v[8:11], v[128:135], v7\n" \
  "s_waitcnt vmcnt(20)\n" \
  "v_smfmac_f32_16x16x64_bf16 v[72:75], v[12:15], v[136:143], v7\n" \
  "s_add_u32 %[stb], %[stb], 0x1000\n" \
  "buffer_load_dwordx4 v[224:227], v0, %[rs], %[stb] offen offset:0 sc1\n" \
  "buffer_load_dwordx4 v[228:231], v0, %[rs], %[stb] offen offset:1024 sc1\n" \
  "s_waitcnt vmcnt(20)\n" \
  "v_smfmac_f32_16x16x64_bf16 v[72:75], v[16:19], v[144:151], v7\n" \
  "s_waitcnt vmcnt(18)\n" \
  "v_smfmac_f32_16x16x64_bf16 v[72:75], v[20:23], v[152:159], v7\n" \
  "buffer_load_dwordx4 v[232:235], v0, %[rs], %[stb] offen offset:2048 sc1\n" \
  "buffer_load_dwordx4 v[236:239], v0, %[rs], %[stb] offen offset:3072 sc1\n" \
  "s_waitcnt vmcnt(18)\n" \
  "v_smfmac_f32_16x16x64_bf16 v[72:75], v[24:27], v[160:167], v7\n" \
  "s_waitcnt vmcnt(16)\n" \
  "v_smfmac_f32_16x16x64_bf16 v[72:75], v[28:31], v[168:175], v7\n" \
  "s_add_u32 %[stb], %[stb], 0x1000\n" \
  "buffer_load_dwordx4 v[240:243], v0, %[rs], %[stb] offen offset:0 sc1\n" \
  "buffer_load_dwordx4 v[244:247], v0, %[rs], %[stb] offen offset:1024 sc1\n" \
  "s_waitcnt vmcnt(16)\n" \
  "v_smfmac_f32_16x16x64_bf16 v[72:75], v[32:35], v[176:183], v7\n" \
  "s_waitcnt vmcnt(14)\n" \
  "v_smfmac_f32_16x16x64_bf16 v[72:75], v[36:39], v[184:191], v7\n" \
  "buffer_load_dwordx4 v[248:251], v0, %[rs], %[stb] offen offset:2048 sc1\n" \
  "buffer_load_dwordx4 v[252:255], v0, %[rs], %[stb] offen offset:3072 sc1\n" \
  "s_nop 7\n" \
  "v_add_f32 v84, v72, v73\n" \
  "v_add_f32 v85, v74, v75\n" \
  "v_pk_mul_f32 v[84:85], v[84:85], %[sc]\n" \
  "v_cvt_pk_bf16_f32 v84, v84, v85\n" \
  "ds_write_b16 v2, v84 offset:10240\n" \
  "ds_write_b16_d16_hi v2, v84 offset:10272\n" \
  "s_waitcnt lgkmcnt(0)\n" \
  "s_barrier\n" \
  "ds_read2st64_b32 v[96:97], v4 offset0:72 offset1:84\n" \
  "ds_read2st64_b32 v[98:99], v4 offset0:96 offset1:108\n" \
  "ds_read2st64_b64 v[88:91], v3 offset0:32 offset1:38\n" \
  "ds_read2st64_b64 v[92:95], v3 offset0:44 offset1:50\n" \
  "s_waitcnt lgkmcnt(2)\n" \
  "v_pk_add_f32 v[96:97], v[96:97], v[98:99]\n" \
  "v_add_f32 v96, v96, v97\n" \
  "v_rcp_f32 v98, v96\n" \
  "v_add_u32 v96, 0xde800000, v96\n" \
  "v_cmp_lt_u32 vcc, 0x3c000000, v96\n" \
  "s_or_b64 %[bad], %[bad], vcc\n" \
  "s_waitcnt lgkmcnt(0)\n" \
  "v_pk_add_f32 v[88:89], v[88:89], v[90:91]\n" \
  "v_pk_add_f32 v[92:93], v[92:93], v[94:95]\n" \
  "v_pk_add_f32 v[88:89], v[88:89], v[92:93]\n" \
  "v_add_f32 v90, v88, v89\n" \
  "v_pk_mul_f32 v[88:89], v[88:89], v[98:99] op_sel_hi:[1,0]\n" \
  "v_cmp_class_f32 vcc, v90, %[cls]\n" \
  "v_cvt_pk_bf16_f32 v88, v88, v89\n" \
  "s_or_b64 %[bad], %[bad], vcc\n" \
  "ds_write_b32 v5, v88 offset:59392\n" \
  "s_add_u32 %[stb], %[stb], 0x1000\n" \
  "buffer_load_dwordx4 v[128:131], v0, %[rs], %[stb] offen offset:0 sc1\n" \
  "buffer_load_dwordx4 v[132:135], v0, %[rs], %[stb] offen offset:1024 sc1\n" \
  "buffer_load_dwordx4 v[136:139], v0, %[rs], %[stb] offen offset:2048 sc1\n" \
  "buffer_load_dwordx4 v[140:143], v0, %[rs], %[stb] offen offset:3072 sc1\n" \
  "s_add_u32 %[stb], %[stb], 0x1000\n" \
  "buffer_load_dwordx4 v[144:147], v0, %[rs], %[stb] offen offset:0 sc1\n" \
  "buffer_load_dwordx4 v[148:151], v0, %[rs], %[stb] offen offset:1024 sc1\n" \
  "buffer_load_dwordx4 v[152:155], v0, %[rs], %[stb] offen offset:2048 sc1\n" \
  "buffer_load_dwordx4 v[156:159], v0, %[rs], %[stb] offen offset:3072 sc1\n" \
  "v_mov_b64 v[72:73], 0\n" \
  "v_mov_b64 v[74:75], 0\n" \
  "s_nop 1\n" \
  "s_waitcnt vmcnt(22)\n" \
  "v_smfmac_f32_16x16x64_bf16 v[72:75], v[8:11], v[192:199], v7\n" \
  "s_add_u32 %[stb], %[stb], 0x1000\n" \
  "buffer_load_dwordx4 v[160:163], v0, %[rs], %[stb] offen offset:0 sc1\n" \
  "buffer_load_dwordx4 v[164:167], v0, %[rs], %[stb] offen offset:1024 sc1\n" \
  "s_waitcnt vmcnt(22)\n" \
  "v_smfmac_f32_16x16x64_bf16 v[72:75], v[12:15], v[200:207], v7\n" \
  "buffer_load_dwordx4 v[168:171], v0, %[rs], %[stb] offen offset:2048 sc1\n" \
  "buffer_load_dwordx4 v[172:175], v0, %[rs], %[stb] offen offset:3072 sc1\n" \
  "s_waitcnt vmcnt(22)\n" \
  "v_smfmac_f32_16x16x64_bf16 v[72:75], v[16:19], v[208:215], v7\n" \
  "s_add_u32 %[stb], %[stb], 0x1000\n" \
  "buffer_load_dwordx4 v[176:179], v0, %[rs], %[stb] offen offset:0 sc1\n" \
  "buffer_load_dwordx4 v[180:183], v0, %[rs], %[stb] offen offset:1024 sc1\n" \
  "s_waitcnt vmcnt(22)\n" \
  "v_smfmac_f32_16x16x64_bf16 v[72:75], v[20:23], v[216:223], v7\n" \
  "buffer_load_dwordx4 v[184:187], v0, %[rs], %[stb] offen offset:2048 sc1\n" \
  "buffer_load_dwordx4 v[188:191], v0, %[rs], %[stb] offen offset:3072 sc1\n" \
  "s_waitcnt vmcnt(22)\n" \
  "v_smfmac_f32_16x16x64_bf16 v[72:75], v[24:27], v[224:231], v7\n" \
  "s_add_u32 %[stb], %[stb], 0x1000\n" \
  "buffer_load_dwordx4 v[192:195], v0, %[rs], %[stb] offen offset:0 sc1\n" \
  "buffer_load_dwordx4 v[196:199], v0, %[rs], %[stb] offen offset:1024 sc1\n" \
  "s_waitcnt vmcnt(22)\n" \
  "v_smfmac_f32_16x16x64_bf16 v[72:75], v[28:31], v[232:239], v7\n" \
  "buffer_load_dwordx4 v[200:203], v0, %[rs], %[stb] offen offset:2048 sc1\n" \
  "buffer_load_dwordx4 v[204:207], v0, %[rs], %[stb] offen offset:3072 sc1\n" \
  "s_waitcnt vmcnt(22)\n" \
  "v_smfmac_f32_16x16x64_bf16 v[72:75], v[32:35], v[240:247], v7\n" \
  "s_add_u32 %[stb], %[stb], 0x1000\n" \
  "buffer_load_dwordx4 v[208:211], v0, %[rs], %[stb] offen offset:0 sc1\n" \
  "buffer_load_dwordx4 v[212:215], v0, %[rs], %[stb] offen offset:1024 sc1\n" \
  "s_waitcnt vmcnt(22)\n" \
  "v_smfmac_f32_16x16x64_bf16 v[72:75], v[36:39], v[248:255], v7\n" \
  "buffer_load_dwordx4 v[216:219], v0, %[rs], %[stb] offen offset:2048 sc1\n" \
  "buffer_load_dwordx4 v[220:223], v0, %[rs], %[stb] offen offset:3072 sc1\n" \
  "s_nop 7\n" \
  "v_add_f32 v84, v72, v73\n" \
  "v_add_f32 v85, v74, v75\n" \
  "v_pk_mul_f32 v[84:85], v[84:85], %[sc]\n" \
  "v_cvt_pk_bf16_f32 v84, v84, v85\n" \
  "ds_write_b16 v2, v84 offset:11264\n" \
  "ds_write_b16_d16_hi v2, v84 offset:11296\n" \
  "s_waitcnt lgkmcnt(0)\n" \
  "s_barrier\n" \
  "ds_read_b128 v[76:79], v0 offset:59392\n" \
  "ds_read2st64_b32 v[96:97], v4 offset0:120 offset1:132\n" \
  "ds_read2st64_b32 v[98:99], v4 offset0:144 offset1:156\n" \
  "ds_read2st64_b64 v[88:91], v3 offset0:56 offset1:62\n" \
  "ds_read2st64_b64 v[92:95], v3 offset0:68 offset1:74\n" \
  "s_waitcnt lgkmcnt(2)\n" \
  "v_pk_add_f32 v[96:97], v[96:97], v[98:99]\n" \
  "v_add_f32 v96, v96, v97\n" \
  "v_rcp_f32 v98, v96\n" \
  "v_add_u32 v96, 0xde800000, v96\n" \
  "v_cmp_lt_u32 vcc, 0x3c000000, v96\n" \
  "s_or_b64 %[bad], %[bad], vcc\n" \
  "s_waitcnt lgkmcnt(0)\n" \
  "v_pk_add_f32 v[88:89], v[88:89], v[90:91]\n" \
  "v_pk_add_f32 v[92:93], v[92:93], v[94:95]\n" \
  "v_pk_add_f32 v[88:89], v[88:89], v[92:93]\n" \
  "v_add_f32 v90, v88, v89\n" \
  "v_pk_mul_f32 v[88:89], v[88:89], v[98:99] op_sel_hi:[1,0]\n" \
  "v_cmp_class_f32 vcc, v90, %[cls]\n" \
  "v_cvt_pk_bf16_f32 v88, v88, v89\n" \
  "s_or_b64 %[bad], %[bad], vcc\n" \
  "ds_write_b32 v5, v88 offset:60416\n" \
  "s_add_u32 %[stb], %[stb], 0x1000\n" \
  "buffer_load_dwordx4 v[224:227], v0, %[rs], %[stb] offen offset:0 sc1\n" \
  "buffer_load_dwordx4 v[228:231], v0, %[rs], %[stb] offen offset:1024 sc1\n" \
  "buffer_load_dwordx4 v[232:235], v0, %[rs], %[stb] offen offset:2048 sc1\n" \
  "buffer_load_dwordx4 v[236:239], v0, %[rs], %[stb] offen offset:3072 sc1\n" \
  "s_add_u32 %[stb], %[stb], 0x1000\n" \
  "buffer_load_dwordx4 v[240:243], v0, %[rs], %[stb] offen offset:0 sc1\n" \
  "buffer_load_dwordx4 v[244:247], v0, %[rs], %[stb] offen offset:1024 sc1\n" \
  "buffer_load_dwordx4 v[248:251], v0, %[rs], %[stb] offen offset:2048 sc1\n" \
  "buffer_load_dwordx4 v[252:255], v0, %[rs], %[stb] offen offset:3072 sc1\n" \
  "s_waitcnt vmcnt(30)\n" \
  "v_smfmac_f32_16x16x64_bf16 v[40:43], v[76:79], v[128:135], v7\n" \
  "s_add_u32 %[stb], %[stb], 0x1000\n" \
  "buffer_load_dwordx4 v[128:131], v0, %[rs], %[stb] offen offset:0 sc1\n" \
  "buffer_load_dwordx4 v[132:135], v0, %[rs], %[stb] offen offset:1024 sc1\n" \
  "s_waitcnt vmcnt(30)\n" \
  "v_smfmac_f32_16x16x64_bf16 v[44:47], v[76:79], v[136:143], v7\n" \
  "buffer_load_dwordx4 v[136:139], v0, %[rs], %[stb] offen offset:2048 sc1\n" \
  "buffer_load_dwordx4 v[140:143], v0, %[rs], %[stb] offen offset:3072 sc1\n" \
  "s_waitcnt vmcnt(30)\n" \
  "v_smfmac_f32_16x16x64_bf16 v[48:51], v[76:79], v[144:151], v7\n" \
  "s_add_u32 %[stb], %[stb], 0x1000\n" \
  "buffer_load_dwordx4 v[144:147], v0, %[rs], %[stb] offen offset:0 sc1\n" \
  "buffer_load_dwordx4 v[148:151], v0, %[rs], %[stb] offen offset:1024 sc1\n" \
  "s_waitcnt vmcnt(30)\n" \
  "v_smfmac_f32_16x16x64_bf16 v[52:55], v[76:79], v[152:159], v7\n" \
  "buffer_load_dwordx4 v[152:155], v0, %[rs], %[stb] offen offset:2048 sc1\n" \
  "buffer_load_dwordx4 v[156:159], v0, %[rs], %[stb] offen offset:3072 sc1\n" \
  "s_waitcnt vmcnt(30)\n" \
  "v_smfmac_f32_16x16x64_bf16 v[56:59], v[76:79], v[160:167], v7\n" \
  "s_add_u32 %[stb], %[stb], 0x1000\n" \
  "buffer_load_dwordx4 v[160:163], v0, %[rs], %[stb] offen offset:0 sc1\n" \
  "buffer_load_dwordx4 v[164:167], v0, %[rs], %[stb] offen offset:1024 sc1\n" \
  "s_waitcnt vmcnt(30)\n" \
  "v_smfmac_f32_16x16x64_bf16 v[60:63], v[76:79], v[168:175], v7\n" \
  "buffer_load_dwordx4 v[168:171], v0, %[rs], %[stb] offen offset:2048 sc1\n" \
  "buffer_load_dwordx4 v[172:175], v0, %[rs], %[stb] offen offset:3072 sc1\n" \
  "s_waitcnt vmcnt(30)\n" \
  "v_smfmac_f32_16x16x64_bf16 v[64:67], v[76:79], v[176:183], v7\n" \
  "s_add_u32 %[stb], %[stb], 0x1000\n" \
  "buffer_load_dwordx4 v[176:179], v0, %[rs], %[stb] offen offset:0 sc1\n" \
  "buffer_load_dwordx4 v[180:183], v0, %[rs], %[stb] offen offset:1024 sc1\n" \
  "s_waitcnt vmcnt(30)\n" \
  "v_smfmac_f32_16x16x64_bf16 v[68:71], v[76:79], v[184:191], v7\n" \
  "buffer_load_dwordx4 v[184:187], v0, %[rs], %[stb] offen offset:2048 sc1\n" \
  "buffer_load_dwordx4 v[188:191], v0, %[rs], %[stb] offen offset:3072 sc1\n" \
  "v_mov_b64 v[72:73], 0\n" \
  "v_mov_b64 v[74:75], 0\n" \
  "s_nop 1\n" \
  "s_waitcnt vmcnt(30)\n" \
  "v_smfmac_f32_16x16x64_bf16 v[72:75], v[8:11], v[192:199], v7\n" \
  "s_add_u32 %[stb], %[stb], 0x1000\n" \
  "buffer_load_dwordx4 v[192:195], v0, %[rs], %[stb] offen offset:0 sc1\n" \
  "buffer_load_dwordx4 v[196:199], v0, %[rs], %[stb] offen offset:1024 sc1\n" \
  "s_waitcnt vmcnt(30)\n" \
  "v_smfmac_f32_16x16x64_bf16 v[72:75], v[12:15], v[200:207], v7\n" \
  "buffer_load_dwordx4 v[200:203], v0, %[rs], %[stb] offen offset:2048 sc1\n" \
  "buffer_load_dwordx4 v[204:207], v0, %[rs], %[stb] offen offset:3072 sc1\n" \
  "s_waitcnt vmcnt(30)\n" \
  "v_smfmac_f32_16x16x64_bf16 v[72:75], v[16:19], v[208:215], v7\n" \
  "s_add_u32 %[stb], %[stb], 0x1000\n" \
  "buffer_load_dwordx4 v[208:211], v0, %[rs], %[stb] offen offset:0 sc1\n" \
  "buffer_load_dwordx4 v[212:215], v0, %[rs], %[stb] offen offset:1024 sc1\n" \
  "s_waitcnt vmcnt(30)\n" \
  "v_smfmac_f32_16x16x64_bf16 v[72:75], v[20:23], v[216:223], v7\n" \
  "buffer_load_dwordx4 v[216:219], v0, %[rs], %[stb] offen offset:2048 sc1\n" \
  "buffer_load_dwordx4 v[220:223], v0, %[rs], %[stb] offen offset:3072 sc1\n" \
  "s_waitcnt vmcnt(30)\n" \
  "v_smfmac_f32_16x16x64_bf16 v[72:75], v[24:27], v[224:231], v7\n" \
  "s_add_u32 %[stb], %[stb], 0x1000\n" \
  "buffer_load_dwordx4 v[224:227], v0, %[rs], %[stb] offen offset:0 sc1\n" \
  "buffer_load_dwordx4 v[228:231], v0, %[rs], %[stb] offen offset:1024 sc1\n" \
  "s_waitcnt vmcnt(30)\n" \
  "v_smfmac_f32_16x16x64_bf16 v[72:75], v[28:31], v[232:239], v7\n" \
  "buffer_load_dwordx4 v[232:235], v0, %[rs], %[stb] offen offset:2048 sc1\n" \
  "buffer_load_dwordx4 v[236:239], v0, %[rs], %[stb] offen offset:3072 sc1\n" \
  "s_waitcnt vmcnt(30)\n" \
  "v_smfmac_f32_16x16x64_bf16 v[72:75], v[32:35], v[240:247], v7\n" \
  "s_add_u32 %[stb], %[stb], 0x1000\n" \
  "buffer_load_dwordx4 v[240:243], v0, %[rs], %[stb] offen offset:0 sc1\n" \
  "buffer_load_dwordx4 v[244:247], v0, %[rs], %[stb] offen offset:1024 sc1\n" \
  "s_waitcnt vmcnt(30)\n" \
  "v_smfmac_f32_16x16x64_bf16 v[72:75], v[36:39], v[248:255], v7\n" \
  "buffer_load_dwordx4 v[248:251], v0, %[rs], %[stb] offen offset:2048 sc1\n" \
  "buffer_load_dwordx4 v[252:255], v0, %[rs], %[stb] offen offset:3072 sc1\n" \
  "s_nop 7\n" \
  "v_add_f32 v84, v72, v73\n" \
  "v_add_f32 v85, v74, v75\n" \
  "v_pk_mul_f32 v[84:85], v[84:85], %[sc]\n" \
  "v_cvt_pk_bf16_f32 v84, v84, v85\n" \
  "ds_write_b16 v2, v84 offset:12288\n" \
  "ds_write_b16_d16_hi v2, v84 offset:12320\n" \
  "s_waitcnt lgkmcnt(0)\n" \
  "s_barrier\n" \
  "ds_read_b128 v[76:79], v0 offset:60416\n" \
  "ds_read2st64_b32 v[96:97], v4 offset0:72 offset1:84\n" \
  "ds_read2st64_b32 v[98:99], v4 offset0:96 offset1:108\n" \
  "ds_read2st64_b64 v[88:91], v3 offset0:32 offset1:38\n" \
  "ds_read2st64_b64 v[92:95], v3 offset0:44 offset1:50\n" \
  "s_waitcnt lgkmcnt(2)\n" \
  "v_pk_add_f32 v[96:97], v[96:97], v[98:99]\n" \
  "v_add_f32 v96, v96, v97\n" \
  "v_rcp_f32 v98, v96\n" \
  "v_add_u32 v96, 0xde800000, v96\n" \
  "v_cmp_lt_u32 vcc, 0x3c000000, v96\n" \
  "s_or_b64 %[bad], %[bad], vcc\n" \
  "s_waitcnt lgkmcnt(0)\n" \
  "v_pk_add_f32 v[88:89], v[88:89], v[90:91]\n" \
  "v_pk_add_f32 v[92:93], v[92:93], v[94:95]\n" \
  "v_pk_add_f32 v[88:89], v[88:89], v[92:93]\n" \
  "v_add_f32 v90, v88, v89\n" \
  "v_pk_mul_f32 v[88:89], v[88:89], v[98:99] op_sel_hi:[1,0]\n" \
  "v_cmp_class_f32 vcc, v90, %[cls]\n" \
  "v_cvt_pk_bf16_f32 v88, v88, v89\n" \
  "s_or_b64 %[bad], %[bad], vcc\n" \
  "ds_write_b32 v5, v88 offset:59392\n" \
  "s_waitcnt vmcnt(30)\n" \
  "v_smfmac_f32_16x16x64_bf16 v[40:43], v[76:79], v[128:135], v7\n" \
  "s_add_u32 %[stb], %[stb], 0x1000\n" \
  "buffer_load_dwordx4 v[128:131], v0, %[rs], %[stb] offen offset:0 sc1\n" \
  "buffer_load_dwordx4 v[132:135], v0, %[rs], %[stb] offen offset:1024 sc1\n" \
  "s_waitcnt vmcnt(30)\n" \
  "v_smfmac_f32_16x16x64_bf16 v[44:47], v[76:79], v[136:143], v7\n" \
  "buffer_load_dwordx4 v[136:139], v0, %[rs], %[stb] offen offset:2048 sc1\n" \
  "buffer_load_dwordx4 v[140:143], v0, %[rs], %[stb] offen offset:3072 sc1\n" \
  "s_waitcnt vmcnt(30)\n" \
  "v_smfmac_f32_16x16x64_bf16 v[48:51], v[76:79], v[144:151], v7\n" \
  "s_add_u32 %[stb], %[stb], 0x1000\n" \
  "buffer_load_dwordx4 v[144:147], v0, %[rs], %[stb] offen offset:0 sc1\n" \
  "buffer_load_dwordx4 v[148:151], v0, %[rs], %[stb] offen offset:1024 sc1\n" \
  "s_waitcnt vmcnt(30)\n" \
  "v_smfmac_f32_16x16x64_bf16 v[52:55], v[76:79], v[152:159], v7\n" \
  "buffer_load_dwordx4 v[152:155], v0, %[rs], %[stb] offen offset:2048 sc1\n" \
  "buffer_load_dwordx4 v[156:159], v0, %[rs], %[stb] offen offset:3072 sc1\n" \
  "s_waitcnt vmcnt(30)\n" \
  "v_smfmac_f32_16x16x64_bf16 v[56:59], v[76:79], v[160:167], v7\n" \
  "s_add_u32 %[stb], %[stb], 0x1000\n" \
  "buffer_load_dwordx4 v[160:163], v0, %[rs], %[stb] offen offset:0 sc1\n" \
  "buffer_load_dwordx4 v[164:167], v0, %[rs], %[stb] offen offset:1024 sc1\n" \
  "s_waitcnt vmcnt(30)\n" \
  "v_smfmac_f32_16x16x64_bf16 v[60:63], v[76:79], v[168:175], v7\n" \
  "buffer_load_dwordx4 v[168:171], v0, %[rs], %[stb] offen offset:2048 sc1\n" \
  "buffer_load_dwordx4 v[172:175], v0, %[rs], %[stb] offen offset:3072 sc1\n" \
  "s_waitcnt vmcnt(30)\n" \
  "v_smfmac_f32_16x16x64_bf16 v[64:67], v[76:79], v[176:183], v7\n" \
  "s_add_u32 %[stb], %[stb], 0x1000\n" \
  "buffer_load_dwordx4 v[176:179], v0, %[rs], %[stb] offen offset:0 sc1\n" \
  "buffer_load_dwordx4 v[180:183], v0, %[rs], %[stb] offen offset:1024 sc1\n" \
  "s_waitcnt vmcnt(30)\n" \
  "v_smfmac_f32_16x16x64_bf16 v[68:71], v[76:79], v[184:191], v7\n" \
  "buffer_load_dwordx4 v[184:187], v0, %[rs], %[stb] offen offset:2048 sc1\n" \
  "buffer_load_dwordx4 v[188:191], v0, %[rs], %[stb] offen offset:3072 sc1\n" \
  "v_mov_b64 v[72:73], 0\n" \
  "v_mov_b64 v[74:75], 0\n" \
  "s_nop 1\n" \
  "s_waitcnt vmcnt(30)\n" \
  "v_smfmac_f32_16x16x64_bf16 v[72:75], v[8:11], v[192:199], v7\n" \
  "s_add_u32 %[stb], %[stb], 0x1000\n" \
  "buffer_load_dwordx4 v[192:195], v0, %[rs], %[stb] offen offset:0 sc1\n" \
  "buffer_load_dwordx4 v[196:199], v0, %[rs], %[stb] offen offset:1024 sc1\n" \
  "s_waitcnt vmcnt(30)\n" \
  "v_smfmac_f32_16x16x64_bf16 v[72:75], v[12:15], v[200:207], v7\n" \
  "buffer_load_dwordx4 v[200:203], v0, %[rs], %[stb] offen offset:2048 sc1\n" \
  "buffer_load_dwordx4 v[204:207], v0, %[rs], %[stb] offen offset:3072 sc1\n" \
  "s_waitcnt vmcnt(30)\n" \
  "v_smfmac_f32_16x16x64_bf16 v[72:75], v[16:19], v[208:215], v7\n" \
  "s_add_u32 %[stb], %[stb], 0x1000\n" \
  "buffer_load_dwordx4 v[208:211], v0, %[rs], %[stb] offen offset:0 sc1\n" \
  "buffer_load_dwordx4 v[212:215], v0, %[rs], %[stb] offen offset:1024 sc1\n" \
  "s_waitcnt vmcnt(30)\n" \
  "v_smfmac_f32_16x16x64_bf16 v[72:75], v[20:23], v[216:223], v7\n" \
  "buffer_load_dwordx4 v[216:219], v0, %[rs], %[stb] offen offset:2048 sc1\n" \
  "buffer_load_dwordx4 v[220:223], v0, %[rs], %[stb] offen offset:3072 sc1\n" \
  "s_waitcnt vmcnt(30)\n" \
  "v_smfmac_f32_16x16x64_bf16 v[72:75], v[24:27], v[224:231], v7\n" \
  "s_add_u32 %[stb], %[stb], 0x1000\n" \
  "buffer_load_dwordx4 v[224:227], v0, %[rs], %[stb] offen offset:0 sc1\n" \
  "buffer_load_dwordx4 v[228:231], v0, %[rs], %[stb] offen offset:1024 sc1\n" \
  "s_waitcnt vmcnt(30)\n" \
  "v_smfmac_f32_16x16x64_bf16 v[72:75], v[28:31], v[232:239], v7\n" \
  "buffer_load_dwordx4 v[232:235], v0, %[rs], %[stb] offen offset:2048 sc1\n" \
  "buffer_load_dwordx4 v[236:239], v0, %[rs], %[stb] offen offset:3072 sc1\n" \
  "s_waitcnt vmcnt(30)\n" \
  "v_smfmac_f32_16x16x64_bf16 v[72:75], v[32:35], v[240:247], v7\n" \
  "s_add_u32 %[stb], %[stb], 0x1000\n" \
  "buffer_load_dwordx4 v[240:243], v0, %[rs], %[stb] offen offset:0 sc1\n" \
  "buffer_load_dwordx4 v[244:247], v0, %[rs], %[stb] offen offset:1024 sc1\n" \
  "s_waitcnt vmcnt(30)\n" \
  "v_smfmac_f32_16x16x64_bf16 v[72:75], v[36:39], v[248:255], v7\n" \
  "buffer_load_dwordx4 v[248:251], v0, %[rs], %[stb] offen offset:2048 sc1\n" \
  "buffer_load_dwordx4 v[252:255], v0, %[rs], %[stb] offen offset:3072 sc1\n" \
  "s_nop 7\n" \
  "v_add_f32 v84, v72, v73\n" \
  "v_add_f32 v85, v74, v75\n" \
  "v_pk_mul_f32 v[84:85], v[84:85], %[sc]\n" \
  "v_cvt_pk_bf16_f32 v84, v84, v85\n" \
  "ds_write_b16 v2, v84 offset:13312\n" \
  "ds_write_b16_d16_hi v2, v84 offset:13344\n" \
  "s_waitcnt lgkmcnt(0)\n" \
  "s_barrier\n" \
  "ds_read_b128 v[76:79], v0 offset:59392\n" \
  "ds_read2st64_b32 v[96:97], v4 offset0:120 offset1:132\n" \
  "ds_read2st64_b32 v[98:99], v4 offset0:144 offset1:156\n" \
  "ds_read2st64_b64 v[88:91], v3 offset0:56 offset1:62\n" \
  "ds_read2st64_b64 v[92:95], v3 offset0:68 offset1:74\n" \
  "s_waitcnt lgkmcnt(2)\n" \
  "v_pk_add_f32 v[96:97], v[96:97], v[98:99]\n" \
  "v_add_f32 v96, v96, v97\n" \
  "v_rcp_f32 v98, v96\n" \
  "v_add_u32 v96, 0xde800000, v96\n" \
  "v_cmp_lt_u32 vcc, 0x3c000000, v96\n" \
  "s_or_b64 %[bad], %[bad], vcc\n" \
  "s_waitcnt lgkmcnt(0)\n" \
  "v_pk_add_f32 v[88:89], v[88:89], v[90:91]\n" \
  "v_pk_add_f32 v[92:93], v[92:93], v[94:95]\n" \
  "v_pk_add_f32 v[88:89], v[88:89], v[92:93]\n" \
  "v_add_f32 v90, v88, v89\n" \
  "v_pk_mul_f32 v[88:89], v[88:89], v[98:99] op_sel_hi:[1,0]\n" \
  "v_cmp_class_f32 vcc, v90, %[cls]\n" \
  "v_cvt_pk_bf16_f32 v88, v88, v89\n" \
  "s_or_b64 %[bad], %[bad], vcc\n" \
  "ds_write_b32 v5, v88 offset:60416\n" \
  "s_waitcnt vmcnt(30)\n" \
  "v_smfmac_f32_16x16x64_bf16 v[40:43], v[76:79], v[128:135], v7\n" \
  "s_add_u32 %[stb], %[stb], 0x1000\n" \
  "buffer_load_dwordx4 v[128:131], v0, %[rs], %[stb] offen offset:0 sc1\n" \
  "buffer_load_dwordx4 v[132:135], v0, %[rs], %[stb] offen offset:1024 sc1\n" \
  "s_waitcnt vmcnt(30)\n" \
  "v_smfmac_f32_16x16x64_bf16 v[44:47], v[76:79], v[136:143], v7\n" \
  "buffer_load_dwordx4 v[136:139], v0, %[rs], %[stb] offen offset:2048 sc1\n" \
  "buffer_load_dwordx4 v[140:143], v0, %[rs], %[stb] offen offset:3072 sc1\n" \
  "s_waitcnt vmcnt(30)\n" \
  "v_smfmac_f32_16x16x64_bf16 v[48:51], v[76:79], v[144:151], v7\n" \
  "s_add_u32 %[stb], %[stb], 0x1000\n" \
  "buffer_load_dwordx4 v[144:147], v0, %[rs], %[stb] offen offset:0 sc1\n" \
  "buffer_load_dwordx4 v[148:151], v0, %[rs], %[stb] offen offset:1024 sc1\n" \
  "s_waitcnt vmcnt(30)\n" \
  "v_smfmac_f32_16x16x64_bf16 v[52:55], v[76:79], v[152:159], v7\n" \
  "buffer_load_dwordx4 v[152:155], v0, %[rs], %[stb] offen offset:2048 sc1\n" \
  "buffer_load_dwordx4 v[156:159], v0, %[rs], %[stb] offen offset:3072 sc1\n" \
  "s_waitcnt vmcnt(30)\n" \
  "v_smfmac_f32_16x16x64_bf16 v[56:59], v[76:79], v[160:167], v7\n" \
  "s_add_u32 %[stb], %[stb], 0x1000\n" \
  "buffer_load_dwordx4 v[160:163], v0, %[rs], %[stb] offen offset:0 sc1\n" \
  "buffer_load_dwordx4 v[164:167], v0, %[rs], %[stb] offen offset:1024 sc1\n" \
  "s_waitcnt vmcnt(30)\n" \
  "v_smfmac_f32_16x16x64_bf16 v[60:63], v[76:79], v[168:175], v7\n" \
  "buffer_load_dwordx4 v[168:171], v0, %[rs], %[stb] offen offset:2048 sc1\n" \
  "buffer_load_dwordx4 v[172:175], v0, %[rs], %[stb] offen offset:3072 sc1\n" \
  "s_waitcnt vmcnt(30)\n" \
  "v_smfmac_f32_16x16x64_bf16 v[64:67], v[76:79], v[176:183], v7\n" \
  "s_add_u32 %[stb], %[stb], 0x1000\n" \
  "buffer_load_dwordx4 v[176:179], v0, %[rs], %[stb] offen offset:0 sc1\n" \
  "buffer_load_dwordx4 v[180:183], v0, %[rs], %[stb] offen offset:1024 sc1\n" \
  "s_waitcnt vmcnt(30)\n" \
  "v_smfmac_f32_16x16x64_bf16 v[68:71], v[76:79], v[184:191], v7\n" \
  "buffer_load_dwordx4 v[184:187], v0, %[rs], %[stb] offen offset:2048 sc1\n" \
  "buffer_load_dwordx4 v[188:191], v0, %[rs], %[stb] offen offset:3072 sc1\n" \
  "v_mov_b64 v[72:73], 0\n" \
  "v_mov_b64 v[74:75], 0\n" \
  "s_nop 1\n" \
  "s_waitcnt vmcnt(30)\n" \
  "v_smfmac_f32_16x16x64_bf16 v[72:75], v[8:11], v[192:199], v7\n" \
  "s_add_u32 %[stb], %[stb], 0x1000\n" \
  "buffer_load_dwordx4 v[192:195], v0, %[rs], %[stb] offen offset:0 sc1\n" \
  "buffer_load_dwordx4 v[196:199], v0, %[rs], %[stb] offen offset:1024 sc1\n" \
  "s_waitcnt vmcnt(30)\n" \
  "v_smfmac_f32_16x16x64_bf16 v[72:75], v[12:15], v[200:207], v7\n" \
  "buffer_load_dwordx4 v[200:203], v0, %[rs], %[stb] offen offset:2048 sc1\n" \
  "buffer_load_dwordx4 v[204:207], v0, %[rs], %[stb] offen offset:3072 sc1\n" \
  "s_waitcnt vmcnt(30)\n" \
  "v_smfmac_f32_16x16x64_bf16 v[72:75], v[16:19], v[208:215], v7\n" \
  "s_add_u32 %[stb], %[stb], 0x1000\n" \
  "buffer_load_dwordx4 v[208:211], v0, %[rs], %[stb] offen offset:0 sc1\n" \
  "buffer_load_dwordx4 v[212:215], v0, %[rs], %[stb] offen offset:1024 sc1\n" \
  "s_waitcnt vmcnt(30)\n" \
  "v_smfmac_f32_16x16x64_bf16 v[72:75], v[20:23], v[216:223], v7\n" \
  "buffer_load_dwordx4 v[216:219], v0, %[rs], %[stb] offen offset:2048 sc1\n" \
  "buffer_load_dwordx4 v[220:223], v0, %[rs], %[stb] offen offset:3072 sc1\n" \
  "s_waitcnt vmcnt(30)\n" \
  "v_smfmac_f32_16x16x64_bf16 v[72:75], v[24:27], v[224:231], v7\n" \
  "s_add_u32 %[stb], %[stb], 0x1000\n" \
  "buffer_load_dwordx4 v[224:227], v0, %[rs], %[stb] offen offset:0 sc1\n" \
  "buffer_load_dwordx4 v[228:231], v0, %[rs], %[stb] offen offset:1024 sc1\n" \
  "s_waitcnt vmcnt(30)\n" \
  "v_smfmac_f32_16x16x64_bf16 v[72:75], v[28:31], v[232:239], v7\n" \
  "buffer_load_dwordx4 v[232:235], v0, %[rs], %[stb] offen offset:2048 sc1\n" \
  "buffer_load_dwordx4 v[236:239], v0, %[rs], %[stb] offen offset:3072 sc1\n" \
  "s_waitcnt vmcnt(30)\n" \
  "v_smfmac_f32_16x16x64_bf16 v[72:75], v[32:35], v[240:247], v7\n" \
  "s_add_u32 %[stb], %[stb], 0x1000\n" \
  "buffer_load_dwordx4 v[240:243], v0, %[rs], %[stb] offen offset:0 sc1\n" \
  "buffer_load_dwordx4 v[244:247], v0, %[rs], %[stb] offen offset:1024 sc1\n" \
  "s_waitcnt vmcnt(30)\n" \
  "v_smfmac_f32_16x16x64_bf16 v[72:75], v[36:39], v[248:255], v7\n" \
  "buffer_load_dwordx4 v[248:251], v0, %[rs], %[stb] offen offset:2048 sc1\n" \
  "buffer_load_dwordx4 v[252:255], v0, %[rs], %[stb] offen offset:3072 sc1\n" \
  "s_nop 7\n" \
  "v_add_f32 v84, v72, v73\n" \
  "v_add_f32 v85, v74, v75\n" \
  "v_pk_mul_f32 v[84:85], v[84:85], %[sc]\n" \
  "v_cvt_pk_bf16_f32 v84, v84, v85\n" \
  "ds_write_b16 v2, v84 offset:14336\n" \
  "ds_write_b16_d16_hi v2, v84 offset:14368\n" \
  "s_waitcnt lgkmcnt(0)\n" \
  "s_barrier\n" \
  "ds_read_b128 v[76:79], v0 offset:60416\n" \
  "ds_read2st64_b32 v[96:97], v4 offset0:72 offset1:84\n" \
  "ds_read2st64_b32 v[98:99], v4 offset0:96 offset1:108\n" \
  "ds_read2st64_b64 v[88:91], v3 offset0:32 offset1:38\n" \
  "ds_read2st64_b64 v[92:95], v3 offset0:44 offset1:50\n" \
  "s_waitcnt lgkmcnt(2)\n" \
  "v_pk_add_f32 v[96:97], v[96:97], v[98:99]\n" \
  "v_add_f32 v96, v96, v97\n" \
  "v_rcp_f32 v98, v96\n" \
  "v_add_u32 v96, 0xde800000, v96\n" \
  "v_cmp_lt_u32 vcc, 0x3c000000, v96\n" \
  "s_or_b64 %[bad], %[bad], vcc\n" \
  "s_waitcnt lgkmcnt(0)\n" \
  "v_pk_add_f32 v[88:89], v[88:89], v[90:91]\n" \
  "v_pk_add_f32 v[92:93], v[92:93], v[94:95]\n" \
  "v_pk_add_f32 v[88:89], v[88:89], v[92:93]\n" \
  "v_add_f32 v90, v88, v89\n" \
  "v_pk_mul_f32 v[88:89], v[88:89], v[98:99] op_sel_hi:[1,0]\n" \
  "v_cmp_class_f32 vcc, v90, %[cls]\n" \
  "v_cvt_pk_bf16_f32 v88, v88, v89\n" \
  "s_or_b64 %[bad], %[bad], vcc\n" \
  "ds_write_b32 v5, v88 offset:59392\n" \
  "s_waitcnt vmcnt(30)\n" \
  "v_smfmac_f32_16x16x64_bf16 v[40:43], v[76:79], v[128:135], v7\n" \
  "s_add_u32 %[stb], %[stb], 0x1000\n" \
  "buffer_load_dwordx4 v[128:131], v0, %[rs], %[stb] offen offset:0 sc1\n" \
  "buffer_load_dwordx4 v[132:135], v0, %[rs], %[stb] offen offset:1024 sc1\n" \
  "s_waitcnt vmcnt(30)\n" \
  "v_smfmac_f32_16x16x64_bf16 v[44:47], v[76:79], v[136:143], v7\n" \
  "buffer_load_dwordx4 v[136:139], v0, %[rs], %[stb] offen offset:2048 sc1\n" \
  "buffer_load_dwordx4 v[140:143], v0, %[rs], %[stb] offen offset:3072 sc1\n" \
  "s_waitcnt vmcnt(30)\n" \
  "v_smfmac_f32_16x16x64_bf16 v[48:51], v[76:79], v[144:151], v7\n" \
  ".Lpad0_%=:\n" \
  "s_cbranch_execz .Lpad1_%=\n" \
  "s_add_u32 %[stb], %[stb], 0x1000\n" \
  "buffer_load_dwordx4 v[144:147], v0, %[rs], %[stb] offen offset:0 sc1\n" \
  "buffer_load_dwordx4 v[148:151], v0, %[rs], %[stb] offen offset:1024 sc1\n" \
  "s_waitcnt vmcnt(30)\n" \
  "v_smfmac_f32_16x16x64_bf16 v[52:55], v[76:79], v[152:159], v7\n" \
  "buffer_load_dwordx4 v[152:155], v0, %[rs], %[stb] offen offset:2048 sc1\n" \
  "buffer_load_dwordx4 v[156:159], v0, %[rs], %[stb] offen offset:3072 sc1\n" \
  "s_waitcnt vmcnt(30)\n" \
  "v_smfmac_f32_16x16x64_bf16 v[56:59], v[76:79], v[160:167], v7\n" \
  "s_add_u32 %[stb], %[stb], 0x1000\n" \
  "buffer_load_dwordx4 v[160:163], v0, %[rs], %[stb] offen offset:0 sc1\n" \
  "buffer_load_dwordx4 v[164:167], v0, %[rs], %[stb] offen offset:1024 sc1\n" \
  "s_waitcnt vmcnt(30)\n" \
  "v_smfmac_f32_16x16x64_bf16 v[60:63], v[76:79], v[168:175], v7\n" \
  "buffer_load_dwordx4 v[168:171], v0, %[rs], %[stb] offen offset:2048 sc1\n" \
  "buffer_load_dwordx4 v[172:175], v0, %[rs], %[stb] offen offset:3072 sc1\n" \
  "s_waitcnt vmcnt(30)\n" \
  "v_smfmac_f32_16x16x64_bf16 v[64:67], v[76:79], v[176:183], v7\n" \
  "s_add_u32 %[stb], %[stb], 0x1000\n" \
  "buffer_load_dwordx4 v[176:179], v0, %[rs], %[stb] offen offset:0 sc1\n" \
  "buffer_load_dwordx4 v[180:183], v0, %[rs], %[stb] offen offset:1024 sc1\n" \
  "s_waitcnt vmcnt(30)\n" \
  "v_smfmac_f32_16x16x64_bf16 v[68:71], v[76:79], v[184:191], v7\n" \
  "buffer_load_dwordx4 v[184:187], v0, %[rs], %[stb] offen offset:2048 sc1\n" \
  "buffer_load_dwordx4 v[188:191], v0, %[rs], %[stb] offen offset:3072 sc1\n" \
  "v_mov_b64 v[72:73], 0\n" \
  "v_mov_b64 v[74:75], 0\n" \
  "s_nop 1\n" \
  "s_waitcnt vmcnt(30)\n" \
  "v_smfmac_f32_16x16x64_bf16 v[72:75], v[8:11], v[192:199], v7\n" \
  "s_add_u32 %[stb], %[stb], 0x1000\n" \
  "buffer_load_dwordx4 v[192:195], v0, %[rs], %[stb] offen offset:0 sc1\n" \
  "buffer_load_dwordx4 v[196:199], v0, %[rs], %[stb] offen offset:1024 sc1\n" \
  "s_waitcnt vmcnt(30)\n" \
  "v_smfmac_f32_16x16x64_bf16 v[72:75], v[12:15], v[200:207], v7\n" \
  "buffer_load_dwordx4 v[200:203], v0, %[rs], %[stb] offen offset:2048 sc1\n" \
  "buffer_load_dwordx4 v[204:207], v0, %[rs], %[stb] offen offset:3072 sc1\n" \
  "s_waitcnt vmcnt(30)\n" \
  "v_smfmac_f32_16x16x64_bf16 v[72:75], v[16:19], v[208:215], v7\n" \
  "s_add_u32 %[stb], %[stb], 0x1000\n" \
  "buffer_load_dwordx4 v[208:211], v0, %[rs], %[stb] offen offset:0 sc1\n" \
  "buffer_load_dwordx4 v[212:215], v0, %[rs], %[stb] offen offset:1024 sc1\n" \
  "s_waitcnt vmcnt(30)\n" \
  "v_smfmac_f32_16x16x64_bf16 v[72:75], v[20:23], v[216:223], v7\n" \
  "buffer_load_dwordx4 v[216:219], v0, %[rs], %[stb] offen offset:2048 sc1\n" \
  "buffer_load_dwordx4 v[220:223], v0, %[rs], %[stb] offen offset:3072 sc1\n" \
  "s_waitcnt vmcnt(30)\n" \
  "v_smfmac_f32_16x16x64_bf16 v[72:75], v[24:27], v[224:231], v7\n" \
  "s_add_u32 %[stb], %[stb], 0x1000\n" \
  "buffer_load_dwordx4 v[224:227], v0, %[rs], %[stb] offen offset:0 sc1\n" \
  "buffer_load_dwordx4 v[228:231], v0, %[rs], %[stb] offen offset:1024 sc1\n" \
  "s_waitcnt vmcnt(30)\n" \
  "v_smfmac_f32_16x16x64_bf16 v[72:75], v[28:31], v[232:239], v7\n" \
  "buffer_load_dwordx4 v[232:235], v0, %[rs], %[stb] offen offset:2048 sc1\n" \
  "buffer_load_dwordx4 v[236:239], v0, %[rs], %[stb] offen offset:3072 sc1\n" \
  "s_waitcnt vmcnt(30)\n" \
  "v_smfmac_f32_16x16x64_bf16 v[72:75], v[32:35], v[240:247], v7\n" \
  "s_add_u32 %[stb], %[stb], 0x1000\n" \
  "buffer_load_dwordx4 v[240:243], v0, %[rs], %[stb] offen offset:0 sc1\n" \
  "buffer_load_dwordx4 v[244:247], v0, %[rs], %[stb] offen offset:1024 sc1\n" \
  "s_waitcnt vmcnt(30)\n" \
  "v_smfmac_f32_16x16x64_bf16 v[72:75], v[36:39], v[248:255], v7\n" \
  "buffer_load_dwordx4 v[248:251], v0, %[rs], %[stb] offen offset:2048 sc1\n" \
  "buffer_load_dwordx4 v[252:255], v0, %[rs], %[stb] offen offset:3072 sc1\n" \
  "s_nop 7\n" \
  "v_add_f32 v84, v72, v73\n" \
  "v_add_f32 v85, v74, v75\n" \
  "v_pk_mul_f32 v[84:85], v[84:85], %[sc]\n" \
  "v_cvt_pk_bf16_f32 v84, v84, v85\n" \
  "ds_write_b16 v2, v84 offset:15360\n" \
  "ds_write_b16_d16_hi v2, v84 offset:15392\n" \
  "s_waitcnt lgkmcnt(0)\n" \
  "s_barrier\n" \
  "ds_read_b128 v[76:79], v0 offset:59392\n" \
  "ds_read2st64_b32 v[96:97], v4 offset0:120 offset1:132\n" \
  "ds_read2st64_b32 v[98:99], v4 offset0:144 offset1:156\n" \
  "ds_read2st64_b64 v[88:91], v3 offset0:56 offset1:62\n" \
  "ds_read2st64_b64 v[92:95], v3 offset0:68 offset1:74\n" \
  "s_waitcnt lgkmcnt(2)\n" \
  "v_pk_add_f32 v[96:97], v[96:97], v[98:99]\n" \
  "v_add_f32 v96, v96, v97\n" \
  "v_rcp_f32 v98, v96\n" \
  "v_add_u32 v96, 0xde800000, v96\n" \
  "v_cmp_lt_u32 vcc, 0x3c000000, v96\n" \
  "s_or_b64 %[bad], %[bad], vcc\n" \
  "s_waitcnt lgkmcnt(0)\n" \
  "v_pk_add_f32 v[88:89], v[88:89], v[90:91]\n" \
  "v_pk_add_f32 v[92:93], v[92:93], v[94:95]\n" \
  "v_pk_add_f32 v[88:89], v[88:89], v[92:93]\n" \
  "v_add_f32 v90, v88, v89\n" \
  "v_pk_mul_f32 v[88:89], v[88:89], v[98:99] op_sel_hi:[1,0]\n" \
  "v_cmp_class_f32 vcc, v90, %[cls]\n" \
  "v_cvt_pk_bf16_f32 v88, v88, v89\n" \
  "s_or_b64 %[bad], %[bad], vcc\n" \
  "ds_write_b32 v5, v88 offset:60416\n" \
  "s_waitcnt vmcnt(30)\n" \
  "v_smfmac_f32_16x16x64_bf16 v[40:43], v[76:79], v[128:135], v7\n" \
  "s_add_u32 %[stb], %[stb], 0x1000\n" \
  "buffer_load_dwordx4 v[128:131], v0, %[rs], %[stb] offen offset:0 sc1\n" \
  "buffer_load_dwordx4 v[132:135], v0, %[rs], %[stb] offen offset:1024 sc1\n" \
  "s_waitcnt vmcnt(30)\n" \
  "v_smfmac_f32_16x16x64_bf16 v[44:47], v[76:79], v[136:143], v7\n" \
  "buffer_load_dwordx4 v[136:139], v0, %[rs], %[stb] offen offset:2048 sc1\n" \
  "buffer_load_dwordx4 v[140:143], v0, %[rs], %[stb] offen offset:3072 sc1\n" \
  "s_waitcnt vmcnt(30)\n" \
  "v_smfmac_f32_16x16x64_bf16 v[48:51], v[76:79], v[144:151], v7\n" \
  "s_add_u32 %[stb], %[stb], 0x1000\n" \
  "buffer_load_dwordx4 v[144:147], v0, %[rs], %[stb] offen offset:0 sc1\n" \
  "buffer_load_dwordx4 v[148:151], v0, %[rs], %[stb] offen offset:1024 sc1\n" \
  "s_waitcnt vmcnt(30)\n" \
  "v_smfmac_f32_16x16x64_bf16 v[52:55], v[76:79], v[152:159], v7\n" \
  "buffer_load_dwordx4 v[152:155], v0, %[rs], %[stb] offen offset:2048 sc1\n" \
  "buffer_load_dwordx4 v[156:159], v0, %[rs], %[stb] offen offset:3072 sc1\n" \
  "s_waitcnt vmcnt(30)\n" \
  "v_smfmac_f32_16x16x64_bf16 v[56:59], v[76:79], v[160:167], v7\n" \
  "s_add_u32 %[stb], %[stb], 0x1000\n" \
  "buffer_load_dwordx4 v[160:163], v0, %[rs], %[stb] offen offset:0 sc1\n" \
  "buffer_load_dwordx4 v[164:167], v0, %[rs], %[stb] offen offset:1024 sc1\n" \
  "s_waitcnt vmcnt(30)\n" \
  "v_smfmac_f32_16x16x64_bf16 v[60:63], v[76:79], v[168:175], v7\n" \
  "buffer_load_dwordx4 v[168:171], v0, %[rs], %[stb] offen offset:2048 sc1\n" \
  "buffer_load_dwordx4 v[172:175], v0, %[rs], %[stb] offen offset:3072 sc1\n" \
  "s_waitcnt vmcnt(30)\n" \
  "v_smfmac_f32_16x16x64_bf16 v[64:67], v[76:79], v[176:183], v7\n" \
  "s_add_u32 %[stb], %[stb], 0x1000\n" \
  "buffer_load_dwordx4 v[176:179], v0, %[rs], %[stb] offen offset:0 sc1\n" \
  "buffer_load_dwordx4 v[180:183], v0, %[rs], %[stb] offen offset:1024 sc1\n" \
  "s_waitcnt vmcnt(30)\n" \
  "v_smfmac_f32_16x16x64_bf16 v[68:71], v[76:79], v[184:191], v7\n" \
  "buffer_load_dwordx4 v[184:187], v0, %[rs], %[stb] offen offset:2048 sc1\n" \
  "buffer_load_dwordx4 v[188:191], v0, %[rs], %[stb] offen offset:3072 sc1\n" \
  "s_add_u32 %[stb], %[stb], 0x1000\n" \
  "buffer_load_dwordx4 v[8:11], v0, %[rs], %[stb] offen offset:0 sc1\n" \
  "buffer_load_dwordx4 v[12:15], v0, %[rs], %[stb] offen offset:1024 sc1\n" \
  "buffer_load_dwordx4 v[16:19], v0, %[rs], %[stb] offen offset:2048 sc1\n" \
  "buffer_load_dwordx4 v[20:23], v0, %[rs], %[stb] offen offset:3072 sc1\n" \
  "s_add_u32 %[stb], %[stb], 0x1000\n" \
  "buffer_load_dwordx4 v[24:27], v0, %[rs], %[stb] offen offset:0 sc1\n" \
  "buffer_load_dwordx4 v[28:31], v0, %[rs], %[stb] offen offset:1024 sc1\n" \
  "buffer_load_dwordx4 v[32:35], v0, %[rs], %[stb] offen offset:2048 sc1\n" \
  "buffer_load_dwordx4 v[36:39], v0, %[rs], %[stb] offen offset:3072 sc1\n" \
  "s_cmp_eq_u32 %[po1], 0\n" \
  "s_cbranch_scc1 .Lnp_%=\n" \
  "v_mov_b32 v84, -1\n" \
  "v_mov_b32 v85, -1\n" \
  "v_mov_b32 v86, -1\n" \
  "v_mov_b32 v87, -1\n" \
  "buffer_store_dwordx4 v[84:87], v0, %[rs], %[po1] offen sc1\n" \
  "buffer_store_dwordx4 v[84:87], v0, %[rs], %[po1] offen offset:1024 sc1\n" \
  "s_cmp_eq_u32 %[po2], 0\n" \
  "s_cbranch_scc1 .Lnp_%=\n" \
  "buffer_store_dwordx4 v[84:87], v0, %[rs], %[po2] offen sc1\n" \
  ".Lnp_%=:\n" \
  "s_waitcnt lgkmcnt(0)\n" \
  "s_barrier\n" \
  "ds_read_b128 v[76:79], v0 offset:60416\n" \
  "ds_read2st64_b32 v[88:89], v4 offset0:72 offset1:84\n" \
  "ds_read2st64_b32 v[90:91], v4 offset0:96 offset1:108\n" \
  "ds_read_b128 v[92:95], v0 offset:16384\n" \
  "ds_read_b128 v[96:99], v0 offset:17408\n" \
  "ds_read_b128 v[100:103], v0 offset:19456\n" \
  "ds_read_b128 v[104:107], v0 offset:20480\n" \
  "ds_read_b128 v[108:111], v0 offset:22528\n" \
  "ds_read_b128 v[112:115], v0 offset:23552\n" \
  "ds_read_b128 v[116:119], v0 offset:25600\n" \
  "ds_read_b128 v[120:123], v0 offset:26624\n" \
  "s_waitcnt lgkmcnt(8)\n" \
  "v_pk_add_f32 v[88:89], v[88:89], v[90:91]\n" \
  "v_add_f32 v88, v88, v89\n" \
  "v_rcp_f32 v90, v88\n" \
  "v_add_u32 v88, 0xde800000, v88\n" \
  "v_cmp_lt_u32 vcc, 0x3c000000, v88\n" \
  "s_or_b64 %[bad], %[bad], vcc\n" \
  "s_waitcnt lgkmcnt(4)\n" \
  "v_pk_add_f32 v[92:93], v[92:93], v[100:101]\n" \
  "v_pk_add_f32 v[94:95], v[94:95], v[102:103]\n" \
  "v_pk_add_f32 v[96:97], v[96:97], v[104:105]\n" \
  "v_pk_add_f32 v[98:99], v[98:99], v[106:107]\n" \
  "s_waitcnt lgkmcnt(0)\n" \
  "v_pk_add_f32 v[108:109], v[108:109], v[116:117]\n" \
  "v_pk_add_f32 v[110:111], v[110:111], v[118:119]\n" \
  "v_pk_add_f32 v[112:113], v[112:113], v[120:121]\n" \
  "v_pk_add_f32 v[114:115], v[114:115], v[122:123]\n" \
  "v_pk_add_f32 v[92:93], v[92:93], v[108:109]\n" \
  "v_pk_add_f32 v[94:95], v[94:95], v[110:111]\n" \
  "v_pk_add_f32 v[96:97], v[96:97], v[112:113]\n" \
  "v_pk_add_f32 v[98:99], v[98:99], v[114:115]\n" \
  "v_pk_mul_f32 v[100:101], v[92:93], v[90:91] op_sel_hi:[1,0]\n" \
  "v_pk_mul_f32 v[102:103], v[94:95], v[90:91] op_sel_hi:[1,0]\n" \
  "v_pk_mul_f32 v[104:105], v[96:97], v[90:91] op_sel_hi:[1,0]\n" \
  "v_pk_mul_f32 v[106:107], v[98:99], v[90:91] op_sel_hi:[1,0]\n" \
  "v_cvt_pk_bf16_f32 v80, v100, v101\n" \
  "v_cvt_pk_bf16_f32 v81, v102, v103\n" \
  "v_cvt_pk_bf16_f32 v82, v104, v105\n" \
  "v_cvt_pk_bf16_f32 v83, v106, v107\n" \
  "v_pk_add_f32 v[92:93], v[92:93], v[94:95]\n" \
  "v_pk_add_f32 v[96:97], v[96:97], v[98:99]\n" \
  "v_pk_add_f32 v[92:93], v[92:93], v[96:97]\n" \
  "v_add_f32 v92, v92, v93\n" \
  "v_cmp_class_f32 vcc, v92, %[cls]\n" \
  "s_or_b64 %[bad], %[bad], vcc\n" \
  "s_waitcnt vmcnt(38)\n" \
  "v_smfmac_f32_16x16x64_bf16 v[40:43], v[76:79], v[192:199], v7\n" \
  "s_sleep 1\n" \
  "s_waitcnt vmcnt(36)\n" \
  "v_smfmac_f32_16x16x64_bf16 v[44:47], v[76:79], v[200:207], v7\n" \
  "s_sleep 1\n" \
  "s_waitcnt vmcnt(34)\n" \
  "v_smfmac_f32_16x16x64_bf16 v[48:51], v[76:79], v[208:215], v7\n" \
  "s_sleep 1\n" \
  "s_waitcnt vmcnt(32)\n" \
  "v_smfmac_f32_16x16x64_bf16 v[52:55], v[76:79], v[216:223], v7\n" \
  "s_sleep 1\n" \
  "s_waitcnt vmcnt(30)\n" \
  "v_smfmac_f32_16x16x64_bf16 v[56:59], v[76:79], v[224:231], v7\n" \
  "s_add_u32 %[stb], %[stb], 0x1000\n" \
  "buffer_load_dwordx4 v[224:227], v0, %[rs], %[stb] offen offset:0 sc1\n" \
  "buffer_load_dwordx4 v[228:231], v0, %[rs], %[stb] offen offset:1024 sc1\n" \
  "s_waitcnt vmcnt(30)\n" \
  "v_smfmac_f32_16x16x64_bf16 v[60:63], v[76:79], v[232:239], v7\n" \
  "buffer_load_dwordx4 v[232:235], v0, %[rs], %[stb] offen offset:2048 sc1\n" \
  "buffer_load_dwordx4 v[236:239], v0, %[rs], %[stb] offen offset:3072 sc1\n" \
  "s_waitcnt vmcnt(30)\n" \
  "v_smfmac_f32_16x16x64_bf16 v[64:67], v[76:79], v[240:247], v7\n" \
  "s_add_u32 %[stb], %[stb], 0x1000\n" \
  "buffer_load_dwordx4 v[240:243], v0, %[rs], %[stb] offen offset:0 sc1\n" \
  "buffer_load_dwordx4 v[244:247], v0, %[rs], %[stb] offen offset:1024 sc1\n" \
  "s_waitcnt vmcnt(30)\n" \
  "v_smfmac_f32_16x16x64_bf16 v[68:71], v[76:79], v[248:255], v7\n" \
  "buffer_load_dwordx4 v[248:251], v0, %[rs], %[stb] offen offset:2048 sc1\n" \
  "buffer_load_dwordx4 v[252:255], v0, %[rs], %[stb] offen offset:3072 sc1\n" \
  "s_waitcnt vmcnt(30)\n" \
  "v_smfmac_f32_16x16x64_bf16 v[40:43], v[80:83], v[128:135], v7\n" \
  "s_sleep 1\n" \
  "s_waitcnt vmcnt(28)\n" \
  "v_smfmac_f32_16x16x64_bf16 v[44:47], v[80:83], v[136:143], v7\n" \
  "s_sleep 1\n" \
  "s_waitcnt vmcnt(26)\n" \
  "v_smfmac_f32_16x16x64_bf16 v[48:51], v[80:83], v[144:151], v7\n" \
  "s_sleep 1\n" \
  "s_waitcnt vmcnt(24)\n" \
  "v_smfmac_f32_16x16x64_bf16 v[52:55], v[80:83], v[152:159], v7\n" \
  "s_sleep 1\n" \
  "s_waitcnt vmcnt(22)\n" \
  "v_smfmac_f32_16x16x64_bf16 v[56:59], v[80:83], v[160:167], v7\n" \
  "s_sleep 1\n" \
  "s_waitcnt vmcnt(20)\n" \
  "v_smfmac_f32_16x16x64_bf16 v[60:63], v[80:83], v[168:175], v7\n" \
  "s_sleep 1\n" \
  "s_waitcnt vmcnt(18)\n" \
  "v_smfmac_f32_16x16x64_bf16 v[64:67], v[80:83], v[176:183], v7\n" \
  "s_sleep 1\n" \
  "s_waitcnt vmcnt(16)\n" \
  "v_smfmac_f32_16x16x64_bf16 v[68:71], v[80:83], v[184:191], v7\n" \
  "s_sleep 1\n" \
  "s_waitcnt vmcnt(0)\n" \
  "s_cmp_eq_u32 %[po1], 0\n" \
  "s_cbranch_scc1 .Lnq_%=\n" \
  "s_sub_u32 %[stb], %[stb], 0x3f000\n" \
  "buffer_load_dwordx4 v[128:131], v0, %[rs], %[stb] offen offset:0 sc1\n" \
  "buffer_load_dwordx4 v[132:135], v0, %[rs], %[stb] offen offset:1024 sc1\n" \
  "buffer_load_dwordx4 v[136:139], v0, %[rs], %[stb] offen offset:2048 sc1\n" \
  "buffer_load_dwordx4 v[140:143], v0, %[rs], %[stb] offen offset:3072 sc1\n" \
  "s_add_u32 %[stb], %[stb], 0x1000\n" \
  "buffer_load_dwordx4 v[144:147], v0, %[rs], %[stb] offen offset:0 sc1\n" \
  "buffer_load_dwordx4 v[148:151], v0, %[rs], %[stb] offen offset:1024 sc1\n" \
  "buffer_load_dwordx4 v[152:155], v0, %[rs], %[stb] offen offset:2048 sc1\n" \
  "buffer_load_dwordx4 v[156:159], v0, %[rs], %[stb] offen offset:3072 sc1\n" \
  "s_add_u32 %[stb], %[stb], 0x1000\n" \
  "buffer_load_dwordx4 v[160:163], v0, %[rs], %[stb] offen offset:0 sc1\n" \
  "buffer_load_dwordx4 v[164:167], v0, %[rs], %[stb] offen offset:1024 sc1\n" \
  "buffer_load_dwordx4 v[168:171], v0, %[rs], %[stb] offen offset:2048 sc1\n" \
  "buffer_load_dwordx4 v[172:175], v0, %[rs], %[stb] offen offset:3072 sc1\n" \
  "s_add_u32 %[stb], %[stb], 0x1000\n" \
  "buffer_load_dwordx4 v[176:179], v0, %[rs], %[stb] offen offset:0 sc1\n" \
  "buffer_load_dwordx4 v[180:183], v0, %[rs], %[stb] offen offset:1024 sc1\n" \
  "buffer_load_dwordx4 v[184:187], v0, %[rs], %[stb] offen offset:2048 sc1\n" \
  "buffer_load_dwordx4 v[188:191], v0, %[rs], %[stb] offen offset:3072 sc1\n" \
  ".Lnq_%=:\n" \
  "v_add_u32 v84, %[xso], v0\n" \
  "ds_read_b128 v[212:215], v84\n" \
  "ds_read_b128 v[216:219], v84 offset:1024\n" \
  "ds_read_b128 v[220:223], v84 offset:2048\n" \
  "v_add_u32 v85, 0x15000, v0\n" \
  "ds_read_b128 v[192:195], v85 offset:0\n" \
  "ds_read_b128 v[196:199], v85 offset:1024\n" \
  "ds_read_b128 v[200:203], v85 offset:2048\n" \
  "ds_read_b128 v[204:207], v85 offset:3072\n" \
  "ds_read_b128 v[208:211], v85 offset:4096\n" \
  "s_waitcnt lgkmcnt(0)\n" \
  "s_barrier\n" \
  "ds_read2st64_b32 v[88:89], v4 offset0:120 offset1:132\n" \
  "ds_read2st64_b32 v[90:91], v4 offset0:144 offset1:156\n" \
  "ds_read_b128 v[92:95], v0 offset:28672\n" \
  "ds_read_b128 v[96:99], v0 offset:29696\n" \
  "ds_read_b128 v[100:103], v0 offset:31744\n" \
  "ds_read_b128 v[104:107], v0 offset:32768\n" \
  "ds_read_b128 v[108:111], v0 offset:34816\n" \
  "ds_read_b128 v[112:115], v0 offset:35840\n" \
  "ds_read_b128 v[116:119], v0 offset:37888\n" \
  "ds_read_b128 v[120:123], v0 offset:38912\n" \
  "s_waitcnt lgkmcnt(8)\n" \
  "v_pk_add_f32 v[88:89], v[88:89], v[90:91]\n" \
  "v_add_f32 v88, v88, v89\n" \
  "v_rcp_f32 v90, v88\n" \
  "v_add_u32 v88, 0xde800000, v88\n" \
  "v_cmp_lt_u32 vcc, 0x3c000000, v88\n" \
  "s_or_b64 %[bad], %[bad], vcc\n" \
  "s_waitcnt lgkmcnt(4)\n" \
  "v_pk_add_f32 v[92:93], v[92:93], v[100:101]\n" \
  "v_pk_add_f32 v[94:95], v[94:95], v[102:103]\n" \
  "v_pk_add_f32 v[96:97], v[96:97], v[104:105]\n" \
  "v_pk_add_f32 v[98:99], v[98:99], v[106:107]\n" \
  "s_waitcnt lgkmcnt(0)\n" \
  "v_pk_add_f32 v[108:109], v[108:109], v[116:117]\n" \
  "v_pk_add_f32 v[110:111], v[110:111], v[118:119]\n" \
  "v_pk_add_f32 v[112:113], v[112:113], v[120:121]\n" \
  "v_pk_add_f32 v[114:115], v[114:115], v[122:123]\n" \
  "v_pk_add_f32 v[92:93], v[92:93], v[108:109]\n" \
  "v_pk_add_f32 v[94:95], v[94:95], v[110:111]\n" \
  "v_pk_add_f32 v[96:97], v[96:97], v[112:113]\n" \
  "v_pk_add_f32 v[98:99], v[98:99], v[114:115]\n" \
  "v_pk_mul_f32 v[100:101], v[92:93], v[90:91] op_sel_hi:[1,0]\n" \
  "v_pk_mul_f32 v[102:103], v[94:95], v[90:91] op_sel_hi:[1,0]\n" \
  "v_pk_mul_f32 v[104:105], v[96:97], v[90:91] op_sel_hi:[1,0]\n" \
  "v_pk_mul_f32 v[106:107], v[98:99], v[90:91] op_sel_hi:[1,0]\n" \
  "v_cvt_pk_bf16_f32 v80, v100, v101\n" \
  "v_cvt_pk_bf16_f32 v81, v102, v103\n" \
  "v_cvt_pk_bf16_f32 v82, v104, v105\n" \
  "v_cvt_pk_bf16_f32 v83, v106, v107\n" \
  "s_nop 1\n" \
  "v_smfmac_f32_16x16x64_bf16 v[40:43], v[80:83], v[8:15], v7\n" \
  "v_smfmac_f32_16x16x64_bf16 v[44:47], v[80:83], v[16:23], v7\n" \
  "v_smfmac_f32_16x16x64_bf16 v[48:51], v[80:83], v[24:31], v7\n" \
  "v_smfmac_f32_16x16x64_bf16 v[52:55], v[80:83], v[32:39], v7\n" \
  "v_smfmac_f32_16x16x64_bf16 v[56:59], v[80:83], v[224:231], v7\n" \
  "v_smfmac_f32_16x16x64_bf16 v[60:63], v[80:83], v[232:239], v7\n" \
  "v_smfmac_f32_16x16x64_bf16 v[64:67], v[80:83], v[240:247], v7\n" \
  "v_smfmac_f32_16x16x64_bf16 v[68:71], v[80:83], v[248:255], v7\n" \
  "s_nop 0\n" \
  "v_add_f32 v108, v40, v41\n" \
  "v_add_f32 v112, v42, v43\n" \
  "v_add_f32 v109, v44, v45\n" \
  "v_add_f32 v113, v46, v47\n" \
  "v_add_f32 v110, v48, v49\n" \
  "v_add_f32 v114, v50, v51\n" \
  "v_add_f32 v111, v52, v53\n" \
  "v_add_f32 v115, v54, v55\n" \
  "ds_write_b128 v6, v[108:111] offset:40960\n" \
  "ds_write_b128 v6, v[112:115] offset:43136\n" \
  "v_add_f32 v116, v56, v57\n" \
  "v_add_f32 v120, v58, v59\n" \
  "v_add_f32 v117, v60, v61\n" \
  "v_add_f32 v121, v62, v63\n" \
  "v_add_f32 v118, v64, v65\n" \
  "v_add_f32 v122, v66, v67\n" \
  "v_add_f32 v119, v68, v69\n" \
  "v_add_f32 v123, v70, v71\n" \
  "ds_write_b128 v6, v[116:119] offset:41088\n" \
  "ds_write_b128 v6, v[120:123] offset:43264\n" \
  "v_pk_add_f32 v[92:93], v[92:93], v[94:95]\n" \
  "v_pk_add_f32 v[96:97], v[96:97], v[98:99]\n" \
  "v_pk_add_f32 v[92:93], v[92:93], v[96:97]\n" \
  "v_add_f32 v92, v92, v93\n" \
  "v_cmp_class_f32 vcc, v92, %[cls]\n" \
  "s_or_b64 %[bad], %[bad], vcc\n" \
  "s_setprio 0\n" \
  "s_cmp_eq_u64 %[bad], 0\n" \
  "s_cbranch_scc1 .Lok_%=\n" \
  "v_mov_b32 v84, 91136\n" \
  "ds_write_b32 v84, v84\n" \
  ".Lok_%=:\n" \
  "s_waitcnt vmcnt(0) lgkmcnt(0)\n" \
  "s_branch .Lend_%=\n" \
  ".Lrt1_%=:\n" \
  "s_mov_b32 %[cnt], 0x10000\n" \
  ".Lrl1_%=:\n" \
  "buffer_load_dwordx4 v[16:19], v1, %[rs], %[kv0] offen offset:2048 sc1\n" \
  "buffer_load_dwordx4 v[80:83], v2, %[rs], %[kv0] offen offset:1024 sc1\n" \
  "s_waitcnt vmcnt(0)\n" \
  "v_max_u32 v255, v16, v80\n" \
  "v_cmp_eq_u32 vcc, -1, v255\n" \
  "s_cbranch_vccz .Lbk1_%=\n" \
  "s_sub_u32 %[cnt], %[cnt], 1\n" \
  "s_cmp_lg_u32 %[cnt], 0\n" \
  "s_cbranch_scc1 .Lrl1_%=\n" \
  "s_branch .Lbk1_%=\n" \
  ".Lrt2_%=:\n" \
  "s_mov_b32 %[cnt], 0x10000\n" \
  ".Lrl2_%=:\n" \
  "buffer_load_dwordx4 v[20:23], v1, %[rs], %[kv0] offen offset:3072 sc1\n" \
  "buffer_load_dwordx4 v[88:91], v2, %[rs], %[kv0] offen offset:2048 sc1\n" \
  "s_waitcnt vmcnt(0)\n" \
  "v_max_u32 v255, v20, v88\n" \
  "v_cmp_eq_u32 vcc, -1, v255\n" \
  "s_cbranch_vccz .Lbk2_%=\n" \
  "s_sub_u32 %[cnt], %[cnt], 1\n" \
  "s_cmp_lg_u32 %[cnt], 0\n" \
  "s_cbranch_scc1 .Lrl2_%=\n" \
  "s_branch .Lbk2_%=\n" \
  ".Lrt3_%=:\n" \
  "s_mov_b32 %[cnt], 0x10000\n" \
  ".Lrl3_%=:\n" \
  "buffer_load_dwordx4 v[24:27], v1, %[rs], %[kv1] offen offset:0 sc1\n" \
  "buffer_load_dwordx4 v[96:99], v2, %[rs], %[kv0] offen offset:3072 sc1\n" \
  "s_waitcnt vmcnt(0)\n" \
  "v_max_u32 v255, v24, v96\n" \
  "v_cmp_eq_u32 vcc, -1, v255\n" \
  "s_cbranch_vccz .Lbk3_%=\n" \
  "s_sub_u32 %[cnt], %[cnt], 1\n" \
  "s_cmp_lg_u32 %[cnt], 0\n" \
  "s_cbranch_scc1 .Lrl3_%=\n" \
  "s_branch .Lbk3_%=\n" \
  ".Lrt4_%=:\n" \
  "s_mov_b32 %[cnt], 0x10000\n" \
  ".Lrl4_%=:\n" \
  "buffer_load_dwordx4 v[28:31], v1, %[rs], %[kv1] offen offset:1024 sc1\n" \
  "buffer_load_dwordx4 v[104:107], v2, %[rs], %[kv1] offen offset:0 sc1\n" \
  "s_waitcnt vmcnt(0)\n" \
  "v_max_u32 v255, v28, v104\n" \
  "v_cmp_eq_u32 vcc, -1, v255\n" \
  "s_cbranch_vccz .Lbk4_%=\n" \
  "s_sub_u32 %[cnt], %[cnt], 1\n" \
  "s_cmp_lg_u32 %[cnt], 0\n" \
  "s_cbranch_scc1 .Lrl4_%=\n" \
  "s_branch .Lbk4_%=\n" \
  ".Lrt5_%=:\n" \
  "s_mov_b32 %[cnt], 0x10000\n" \
  ".Lrl5_%=:\n" \
  "buffer_load_dwordx4 v[32:35], v1, %[rs], %[kv1] offen offset:2048 sc1\n" \
  "buffer_load_dwordx4 v[112:115], v2, %[rs], %[kv1] offen offset:1024 sc1\n" \
  "s_waitcnt vmcnt(0)\n" \
  "v_max_u32 v255, v32, v112\n" \
  "v_cmp_eq_u32 vcc, -1, v255\n" \
  "s_cbranch_vccz .Lbk5_%=\n" \
  "s_sub_u32 %[cnt], %[cnt], 1\n" \
  "s_cmp_lg_u32 %[cnt], 0\n" \
  "s_cbranch_scc1 .Lrl5_%=\n" \
  "s_branch .Lbk5_%=\n" \
  ".Lrt6_%=:\n" \
  "s_mov_b32 %[cnt], 0x10000\n" \
  ".Lrl6_%=:\n" \
  "buffer_load_dwordx4 v[36:39], v1, %[rs], %[kv1] offen offset:3072 sc1\n" \
  "buffer_load_dwordx4 v[120:123], v2, %[rs], %[kv1] offen offset:2048 sc1\n" \
  "s_waitcnt vmcnt(0)\n" \
  "v_max_u32 v255, v36, v120\n" \
  "v_cmp_eq_u32 vcc, -1, v255\n" \
  "s_cbranch_vccz .Lbk6_%=\n" \
  "s_sub_u32 %[cnt], %[cnt], 1\n" \
  "s_cmp_lg_u32 %[cnt], 0\n" \
  "s_cbranch_scc1 .Lrl6_%=\n" \
  "s_branch .Lbk6_%=\n" \
  ".Lrt7_%=:\n" \
  "s_mov_b32 %[cnt], 0x10000\n" \
  ".Lrl7_%=:\n" \
  "buffer_load_dwordx4 v[40:43], v1, %[rs], %[kv2] offen offset:0 sc1\n" \
  "buffer_load_dwordx4 v[128:131], v2, %[rs], %[kv1] offen offset:3072 sc1\n" \
  "s_waitcnt vmcnt(0)\n" \
  "v_max_u32 v255, v40, v128\n" \
  "v_cmp_eq_u32 vcc, -1, v255\n" \
  "s_cbranch_vccz .Lbk7_%=\n" \
  "s_sub_u32 %[cnt], %[cnt], 1\n" \
  "s_cmp_lg_u32 %[cnt], 0\n" \
  "s_cbranch_scc1 .Lrl7_%=\n" \
  "s_branch .Lbk7_%=\n" \
  ".Lrt8_%=:\n" \
  "s_mov_b32 %[cnt], 0x10000\n" \
  ".Lrl8_%=:\n" \
  "buffer_load_dwordx4 v[44:47], v1, %[rs], %[kv2] offen offset:1024 sc1\n" \
  "buffer_load_dwordx4 v[136:139], v2, %[rs], %[kv2] offen offset:0 sc1\n" \
  "s_waitcnt vmcnt(0)\n" \
  "v_max_u32 v255, v44, v136\n" \
  "v_cmp_eq_u32 vcc, -1, v255\n" \
  "s_cbranch_vccz .Lbk8_%=\n" \
  "s_sub_u32 %[cnt], %[cnt], 1\n" \
  "s_cmp_lg_u32 %[cnt], 0\n" \
  "s_cbranch_scc1 .Lrl8_%=\n" \
  "s_branch .Lbk8_%=\n" \
  ".Lrt9_%=:\n" \
  "s_mov_b32 %[cnt], 0x10000\n" \
  ".Lrl9_%=:\n" \
  "buffer_load_dwordx4 v[48:51], v1, %[rs], %[kv2] offen offset:2048 sc1\n" \
  "buffer_load_dwordx4 v[144:147], v2, %[rs], %[kv2] offen offset:1024 sc1\n" \
  "s_waitcnt vmcnt(0)\n" \
  "v_max_u32 v255, v48, v144\n" \
  "v_cmp_eq_u32 vcc, -1, v255\n" \
  "s_cbranch_vccz .Lbk9_%=\n" \
  "s_sub_u32 %[cnt], %[cnt], 1\n" \
  "s_cmp_lg_u32 %[cnt], 0\n" \
  "s_cbranch_scc1 .Lrl9_%=\n" \
  "s_branch .Lbk9_%=\n" \
  ".Lrt10_%=:\n" \
  "s_mov_b32 %[cnt], 0x10000\n" \
  ".Lrl10_%=:\n" \
  "buffer_load_dwordx4 v[52:55], v1, %[rs], %[kv2] offen offset:3072 sc1\n" \
  "buffer_load_dwordx4 v[152:155], v2, %[rs], %[kv2] offen offset:2048 sc1\n" \
  "s_waitcnt vmcnt(0)\n" \
  "v_max_u32 v255, v52, v152\n" \
  "v_cmp_eq_u32 vcc, -1, v255\n" \
  "s_cbranch_vccz .Lbk10_%=\n" \
  "s_sub_u32 %[cnt], %[cnt], 1\n" \
  "s_cmp_lg_u32 %[cnt], 0\n" \
  "s_cbranch_scc1 .Lrl10_%=\n" \
  "s_branch .Lbk10_%=\n" \
  ".Lrt11_%=:\n" \
  "s_mov_b32 %[cnt], 0x10000\n" \
  ".Lrl11_%=:\n" \
  "buffer_load_dwordx4 v[56:59], v1, %[rs], %[kv3] offen offset:0 sc1\n" \
  "buffer_load_dwordx4 v[160:163], v2, %[rs], %[kv2] offen offset:3072 sc1\n" \
  "s_waitcnt vmcnt(0)\n" \
  "v_max_u32 v255, v56, v160\n" \
  "v_cmp_eq_u32 vcc, -1, v255\n" \
  "s_cbranch_vccz .Lbk11_%=\n" \
  "s_sub_u32 %[cnt], %[cnt], 1\n" \
  "s_cmp_lg_u32 %[cnt], 0\n" \
  "s_cbranch_scc1 .Lrl11_%=\n" \
  "s_branch .Lbk11_%=\n" \
  ".Lrt12_%=:\n" \
  "s_mov_b32 %[cnt], 0x10000\n" \
  ".Lrl12_%=:\n" \
  "buffer_load_dwordx4 v[60:63], v1, %[rs], %[kv3] offen offset:1024 sc1\n" \
  "buffer_load_dwordx4 v[168:171], v2, %[rs], %[kv3] offen offset:0 sc1\n" \
  "s_waitcnt vmcnt(0)\n" \
  "v_max_u32 v255, v60, v168\n" \
  "v_cmp_eq_u32 vcc, -1, v255\n" \
  "s_cbranch_vccz .Lbk12_%=\n" \
  "s_sub_u32 %[cnt], %[cnt], 1\n" \
  "s_cmp_lg_u32 %[cnt], 0\n" \
  "s_cbranch_scc1 .Lrl12_%=\n" \
  "s_branch .Lbk12_%=\n" \
  ".Lrt13_%=:\n" \
  "s_mov_b32 %[cnt], 0x10000\n" \
  ".Lrl13_%=:\n" \
  "buffer_load_dwordx4 v[64:67], v1, %[rs], %[kv3] offen offset:2048 sc1\n" \
  "buffer_load_dwordx4 v[176:179], v2, %[rs], %[kv3] offen offset:1024 sc1\n" \
  "s_waitcnt vmcnt(0)\n" \
  "v_max_u32 v255, v64, v176\n" \
  "v_cmp_eq_u32 vcc, -1, v255\n" \
  "s_cbranch_vccz .Lbk13_%=\n" \
  "s_sub_u32 %[cnt], %[cnt], 1\n" \
  "s_cmp_lg_u32 %[cnt], 0\n" \
  "s_cbranch_scc1 .Lrl13_%=\n" \
  "s_branch .Lbk13_%=\n" \
  ".Lrt14_%=:\n" \
  "s_mov_b32 %[cnt], 0x10000\n" \
  ".Lrl14_%=:\n" \
  "buffer_load_dwordx4 v[68:71], v1, %[rs], %[kv3] offen offset:3072 sc1\n" \
  "buffer_load_dwordx4 v[184:187], v2, %[rs], %[kv3] offen offset:2048 sc1\n" \
  "s_waitcnt vmcnt(0)\n" \
  "v_max_u32 v255, v68, v184\n" \
  "v_cmp_eq_u32 vcc, -1, v255\n" \
  "s_cbranch_vccz .Lbk14_%=\n" \
  "s_sub_u32 %[cnt], %[cnt], 1\n" \
  "s_cmp_lg_u32 %[cnt], 0\n" \
  "s_cbranch_scc1 .Lrl14_%=\n" \
  "s_branch .Lbk14_%=\n" \
  ".Lrt15_%=:\n" \
  "s_mov_b32 %[cnt], 0x10000\n" \
  ".Lrl15_%=:\n" \
  "buffer_load_dwordx4 v[192:195], v2, %[rs], %[kv3] offen offset:3072 sc1\n" \
  "s_waitcnt vmcnt(0)\n" \
  "v_cmp_eq_u32 vcc, -1, v192\n" \
  "s_cbranch_vccz .Lbk15_%=\n" \
  "s_sub_u32 %[cnt], %[cnt], 1\n" \
  "s_cmp_lg_u32 %[cnt], 0\n" \
  "s_cbranch_scc1 .Lrl15_%=\n" \
  "s_branch .Lbk15_%=\n" \
  ".Lrtp_%=:\n" \
  "s_mov_b32 %[cnt], 0x10000\n" \
  ".Lrlp_%=:\n" \
  "buffer_load_dwordx4 v[8:11], v1, %[rs], %[kv0] offen offset:0 sc1\n" \
  "buffer_load_dwordx4 v[12:15], v1, %[rs], %[kv0] offen offset:1024 sc1\n" \
  "buffer_load_dwordx4 v[72:75], v2, %[rs], %[kv0] offen offset:0 sc1\n" \
  "s_waitcnt vmcnt(0)\n" \
  "v_max3_u32 v255, v8, v12, v72\n" \
  "v_cmp_eq_u32 vcc, -1, v255\n" \
  "s_cbranch_vccz .Lbkp_%=\n" \
  "s_sub_u32 %[cnt], %[cnt], 1\n" \
  "s_cmp_lg_u32 %[cnt], 0\n" \
  "s_cbranch_scc1 .Lrlp_%=\n" \
  "s_branch .Lbkp_%=\n" \
  ".p2align 12\n" \
  ".LA_%=:\n" \
  "v_mov_b32 v253, v0\n" \
  "v_mov_b32 v254, v3\n" \
  "v_mov_b32 v5, v4\n" \
  "v_mov_b64 v[76:77], v[4:5]\n" \
  "v_mov_b64 v[78:79], v[4:5]\n" \
  "v_mov_b64 v[84:85], v[4:5]\n" \
  "v_mov_b64 v[86:87], v[4:5]\n" \
  "v_mov_b64 v[92:93], v[4:5]\n" \
  "v_mov_b64 v[94:95], v[4:5]\n" \
  "v_mov_b64 v[100:101], v[4:5]\n" \
  "v_mov_b64 v[102:103], v[4:5]\n" \
  "v_mov_b64 v[108:109], v[4:5]\n" \
  "v_mov_b64 v[110:111], v[4:5]\n" \
  "v_mov_b64 v[116:117], v[4:5]\n" \
  "v_mov_b64 v[118:119], v[4:5]\n" \
  "v_mov_b64 v[124:125], v[4:5]\n" \
  "v_mov_b64 v[126:127], v[4:5]\n" \
  "v_mov_b64 v[132:133], v[4:5]\n" \
  "v_mov_b64 v[134:135], v[4:5]\n" \
  "v_mov_b64 v[140:141], v[4:5]\n" \
  "v_mov_b64 v[142:143], v[4:5]\n" \
  "v_mov_b64 v[148:149], v[4:5]\n" \
  "v_mov_b64 v[150:151], v[4:5]\n" \
  "v_mov_b64 v[156:157], v[4:5]\n" \
  "v_mov_b64 v[158:159], v[4:5]\n" \
  "v_mov_b64 v[164:165], v[4:5]\n" \
  "v_mov_b64 v[166:167], v[4:5]\n" \
  "v_mov_b64 v[172:173], v[4:5]\n" \
  "v_mov_b64 v[174:175], v[4:5]\n" \
  "v_mov_b64 v[180:181], v[4:5]\n" \
  "v_mov_b64 v[182:183], v[4:5]\n" \
  "v_mov_b64 v[188:189], v[4:5]\n" \
  "v_mov_b64 v[190:191], v[4:5]\n" \
  "v_mov_b64 v[196:197], v[4:5]\n" \
  "v_mov_b64 v[198:199], v[4:5]\n" \
  "s_sleep %[dly]\n" \
  "buffer_load_dwordx4 v[8:11], v1, %[rs], %[kv0] offen offset:0 sc1\n" \
  "buffer_load_dwordx4 v[12:15], v1, %[rs], %[kv0] offen offset:1024 sc1\n" \
  "buffer_load_dwordx4 v[72:75], v2, %[rs], %[kv0] offen offset:0 sc1\n" \
  "buffer_load_dwordx4 v[16:19], v1, %[rs], %[kv0] offen offset:2048 sc1\n" \
  "buffer_load_dwordx4 v[80:83], v2, %[rs], %[kv0] offen offset:1024 sc1\n" \
  "buffer_load_dwordx4 v[20:23], v1, %[rs], %[kv0] offen offset:3072 sc1\n" \
  "buffer_load_dwordx4 v[88:91], v2, %[rs], %[kv0] offen offset:2048 sc1\n" \
  "buffer_load_dwordx4 v[24:27], v1, %[rs], %[kv1] offen offset:0 sc1\n" \
  "buffer_load_dwordx4 v[96:99], v2, %[rs], %[kv0] offen offset:3072 sc1\n" \
  "s_waitcnt lgkmcnt(0)\n" \
  "s_barrier\n" \
  "ds_read_b128 v[248:251], v253 offset:8192\n" \
  "v_mov_b32 v252, 0xdddd\n" \
  "v_mov_b64 v[232:233], 0\n" \
  "v_mov_b64 v[234:235], 0\n" \
  "v_mov_b64 v[236:237], 0\n" \
  "v_mov_b64 v[238:239], 0\n" \
  "v_mov_b64 v[240:241], 0\n" \
  "v_mov_b64 v[242:243], 0\n" \
  "v_mov_b64 v[244:245], 0\n" \
  "v_mov_b64 v[246:247], 0\n" \
  "s_waitcnt vmcnt(6) lgkmcnt(0)\n" \
  "v_max3_u32 v255, v8, v12, v72\n" \
  "v_cmp_eq_u32 vcc, -1, v255\n" \
  "s_cbranch_vccnz .Lrtp_%=\n" \
  ".Lbkp_%=:\n" \
  "v_mfma_f32_32x32x16_bf16 v[200:215], v[8:11], v[248:251], 0\n" \
  "s_nop 11\n" \
  "v_exp_f32 v200, v200\n" \
  "v_exp_f32 v201, v201\n" \
  "buffer_load_dwordx4 v[28:31], v1, %[rs], %[kv1] offen offset:1024 sc1\n" \
  "v_exp_f32 v202, v202\n" \
  "v_permlane16_swap_b32 v72, v76\n" \
  "v_exp_f32 v203, v203\n" \
  "v_permlane16_swap_b32 v73, v77\n" \
  "v_exp_f32 v204, v204\n" \
  "v_mfma_f32_32x32x16_bf16 v[216:231], v[12:15], v[248:251], 0\n" \
  "buffer_load_dwordx4 v[104:107], v2, %[rs], %[kv1] offen offset:0 sc1\n" \
  "v_exp_f32 v205, v205\n" \
  "v_permlane16_swap_b32 v74, v78\n" \
  "v_exp_f32 v206, v206\n" \
  "v_permlane16_swap_b32 v75, v79\n" \
  "v_exp_f32 v207, v207\n" \
  "v_exp_f32 v208, v208\n" \
  "v_exp_f32 v209, v209\n" \
  "v_smfmac_f32_32x32x32_bf16 v[232:247], v[72:75], v[200:207], v252\n" \
  "s_waitcnt vmcnt(6)\n" \
  "v_exp_f32 v210, v210\n" \
  "v_exp_f32 v211, v211\n" \
  "v_max_u32 v255, v16, v80\n" \
  "v_exp_f32 v212, v212\n" \
  "v_exp_f32 v213, v213\n" \
  "v_cmp_eq_u32 vcc, -1, v255\n" \
  "v_exp_f32 v214, v214\n" \
  "v_exp_f32 v215, v215\n" \
  "v_exp_f32 v216, v216\n" \
  "v_exp_f32 v217, v217\n" \
  "v_smfmac_f32_32x32x32_bf16 v[232:247], v[76:79], v[208:215], v252\n" \
  "s_cbranch_vccnz .Lrt1_%=\n" \
  ".Lbk1_%=:\n" \
  "buffer_load_dwordx4 v[32:35], v1, %[rs], %[kv1] offen offset:2048 sc1\n" \
  "v_exp_f32 v218, v218\n" \
  "v_permlane16_swap_b32 v80, v84\n" \
  "v_exp_f32 v219, v219\n" \
  "v_permlane16_swap_b32 v81, v85\n" \
  "v_exp_f32 v220, v220\n" \
  "v_mfma_f32_32x32x16_bf16 v[200:215], v[16:19], v[248:251], 0\n" \
  "buffer_load_dwordx4 v[112:115], v2, %[rs], %[kv1] offen offset:1024 sc1\n" \
  "v_exp_f32 v221, v221\n" \
  "v_permlane16_swap_b32 v82, v86\n" \
  "v_exp_f32 v222, v222\n" \
  "v_permlane16_swap_b32 v83, v87\n" \
  "v_exp_f32 v223, v223\n" \
  "v_exp_f32 v224, v224\n" \
  "v_exp_f32 v225, v225\n" \
  "v_smfmac_f32_32x32x32_bf16 v[232:247], v[80:83], v[216:223], v252\n" \
  "s_waitcnt vmcnt(6)\n" \
  "v_exp_f32 v226, v226\n" \
  "v_exp_f32 v227, v227\n" \
  "v_max_u32 v255, v20, v88\n" \
  "v_exp_f32 v228, v228\n" \
  "v_exp_f32 v229, v229\n" \
  "v_cmp_eq_u32 vcc, -1, v255\n" \
  "v_exp_f32 v230, v230\n" \
  "v_exp_f32 v231, v231\n" \
  "v_exp_f32 v200, v200\n" \
  "v_exp_f32 v201, v201\n" \
  "v_smfmac_f32_32x32x32_bf16 v[232:247], v[84:87], v[224:231], v252\n" \
  "s_cbranch_vccnz .Lrt2_%=\n" \
  ".Lbk2_%=:\n" \
  "buffer_load_dwordx4 v[36:39], v1, %[rs], %[kv1] offen offset:3072 sc1\n" \
  "v_exp_f32 v202, v202\n" \
  "v_permlane16_swap_b32 v88, v92\n" \
  "v_exp_f32 v203, v203\n" \
  "v_permlane16_swap_b32 v89, v93\n" \
  "v_exp_f32 v204, v204\n" \
  "v_mfma_f32_32x32x16_bf16 v[216:231], v[20:23], v[248:251], 0\n" \
  "buffer_load_dwordx4 v[120:123], v2, %[rs], %[kv1] offen offset:2048 sc1\n" \
  "v_exp_f32 v205, v205\n" \
  "v_permlane16_swap_b32 v90, v94\n" \
  "v_exp_f32 v206, v206\n" \
  "v_permlane16_swap_b32 v91, v95\n" \
  "v_exp_f32 v207, v207\n" \
  "v_exp_f32 v208, v208\n" \
  "v_exp_f32 v209, v209\n" \
  "v_smfmac_f32_32x32x32_bf16 v[232:247], v[88:91], v[200:207], v252\n" \
  "s_waitcnt vmcnt(6)\n" \
  "v_exp_f32 v210, v210\n" \
  "v_exp_f32 v211, v211\n" \
  "v_max_u32 v255, v24, v96\n" \
  "v_exp_f32 v212, v212\n" \
  "v_exp_f32 v213, v213\n" \
  "v_cmp_eq_u32 vcc, -1, v255\n" \
  "v_exp_f32 v214, v214\n" \
  "v_exp_f32 v215, v215\n" \
  "v_exp_f32 v216, v216\n" \
  "v_exp_f32 v217, v217\n" \
  "v_smfmac_f32_32x32x32_bf16 v[232:247], v[92:95], v[208:215], v252\n" \
  "s_cbranch_vccnz .Lrt3_%=\n" \
  ".Lbk3_%=:\n" \
  "buffer_load_dwordx4 v[40:43], v1, %[rs], %[kv2] offen offset:0 sc1\n" \
  "v_exp_f32 v218, v218\n" \
  "v_permlane16_swap_b32 v96, v100\n" \
  "v_exp_f32 v219, v219\n" \
  "v_permlane16_swap_b32 v97, v101\n" \
  "v_exp_f32 v220, v220\n" \
  "v_mfma_f32_32x32x16_bf16 v[200:215], v[24:27], v[248:251], 0\n" \
  "buffer_load_dwordx4 v[128:131], v2, %[rs], %[kv1] offen offset:3072 sc1\n" \
  "v_exp_f32 v221, v221\n" \
  "v_permlane16_swap_b32 v98, v102\n" \
  "v_exp_f32 v222, v222\n" \
  "v_permlane16_swap_b32 v99, v103\n" \
  "v_exp_f32 v223, v223\n" \
  "v_exp_f32 v224, v224\n" \
  "v_exp_f32 v225, v225\n" \
  "v_smfmac_f32_32x32x32_bf16 v[232:247], v[96:99], v[216:223], v252\n" \
  "s_waitcnt vmcnt(6)\n" \
  "v_exp_f32 v226, v226\n" \
  "v_exp_f32 v227, v227\n" \
  "v_max_u32 v255, v28, v104\n" \
  "v_exp_f32 v228, v228\n" \
  "v_exp_f32 v229, v229\n" \
  "v_cmp_eq_u32 vcc, -1, v255\n" \
  "v_exp_f32 v230, v230\n" \
  "v_exp_f32 v231, v231\n" \
  "v_exp_f32 v200, v200\n" \
  "v_exp_f32 v201, v201\n" \
  "v_smfmac_f32_32x32x32_bf16 v[232:247], v[100:103], v[224:231], v252\n" \
  "s_cbranch_vccnz .Lrt4_%=\n" \
  ".Lbk4_%=:\n" \
  "buffer_load_dwordx4 v[44:47], v1, %[rs], %[kv2] offen offset:1024 sc1\n" \
  "v_exp_f32 v202, v202\n" \
  "v_permlane16_swap_b32 v104, v108\n" \
  "v_exp_f32 v203, v203\n" \
  "v_permlane16_swap_b32 v105, v109\n" \
  "v_exp_f32 v204, v204\n" \
  "v_mfma_f32_32x32x16_bf16 v[216:231], v[28:31], v[248:251], 0\n" \
  "buffer_load_dwordx4 v[136:139], v2, %[rs], %[kv2] offen offset:0 sc1\n" \
  "v_exp_f32 v205, v205\n" \
  "v_permlane16_swap_b32 v106, v110\n" \
  "v_exp_f32 v206, v206\n" \
  "v_permlane16_swap_b32 v107, v111\n" \
  "v_exp_f32 v207, v207\n" \
  "v_exp_f32 v208, v208\n" \
  "v_exp_f32 v209, v209\n" \
  "v_smfmac_f32_32x32x32_bf16 v[232:247], v[104:107], v[200:207], v252\n" \
  "s_waitcnt vmcnt(6)\n" \
  "v_exp_f32 v210, v210\n" \
  "v_exp_f32 v211, v211\n" \
  "v_max_u32 v255, v32, v112\n" \
  "v_exp_f32 v212, v212\n" \
  "v_exp_f32 v213, v213\n" \
  "v_cmp_eq_u32 vcc, -1, v255\n" \
  "v_exp_f32 v214, v214\n" \
  "v_exp_f32 v215, v215\n" \
  "v_exp_f32 v216, v216\n" \
  "v_exp_f32 v217, v217\n" \
  "v_smfmac_f32_32x32x32_bf16 v[232:247], v[108:111], v[208:215], v252\n" \
  "s_cbranch_vccnz .Lrt5_%=\n" \
  ".Lbk5_%=:\n" \
  "buffer_load_dwordx4 v[48:51], v1, %[rs], %[kv2] offen offset:2048 sc1\n" \
  "v_exp_f32 v218, v218\n" \
  "v_permlane16_swap_b32 v112, v116\n" \
  "v_exp_f32 v219, v219\n" \
  "v_permlane16_swap_b32 v113, v117\n" \
  "v_exp_f32 v220, v220\n" \
  "v_mfma_f32_32x32x16_bf16 v[200:215], v[32:35], v[248:251], 0\n" \
  "buffer_load_dwordx4 v[144:147], v2, %[rs], %[kv2] offen offset:1024 sc1\n" \
  "v_exp_f32 v221, v221\n" \
  "v_permlane16_swap_b32 v114, v118\n" \
  "v_exp_f32 v222, v222\n" \
  "v_permlane16_swap_b32 v115, v119\n" \
  "v_exp_f32 v223, v223\n" \
  "v_exp_f32 v224, v224\n" \
  "v_exp_f32 v225, v225\n" \
  "v_smfmac_f32_32x32x32_bf16 v[232:247], v[112:115], v[216:223], v252\n" \
  "s_waitcnt vmcnt(6)\n" \
  "v_exp_f32 v226, v226\n" \
  "v_exp_f32 v227, v227\n" \
  "v_max_u32 v255, v36, v120\n" \
  "v_exp_f32 v228, v228\n" \
  "v_exp_f32 v229, v229\n" \
  "v_cmp_eq_u32 vcc, -1, v255\n" \
  "v_exp_f32 v230, v230\n" \
  "v_exp_f32 v231, v231\n" \
  "v_exp_f32 v200, v200\n" \
  "v_exp_f32 v201, v201\n" \
  "v_smfmac_f32_32x32x32_bf16 v[232:247], v[116:119], v[224:231], v252\n" \
  "s_cbranch_vccnz .Lrt6_%=\n" \
  ".Lbk6_%=:\n" \
  "buffer_load_dwordx4 v[52:55], v1, %[rs], %[kv2] offen offset:3072 sc1\n" \
  "v_exp_f32 v202, v202\n" \
  "v_permlane16_swap_b32 v120, v124\n" \
  "v_exp_f32 v203, v203\n" \
  "v_permlane16_swap_b32 v121, v125\n" \
  "v_exp_f32 v204, v204\n" \
  "v_mfma_f32_32x32x16_bf16 v[216:231], v[36:39], v[248:251], 0\n" \
  "buffer_load_dwordx4 v[152:155], v2, %[rs], %[kv2] offen offset:2048 sc1\n" \
  "v_exp_f32 v205, v205\n" \
  "v_permlane16_swap_b32 v122, v126\n" \
  "v_exp_f32 v206, v206\n" \
  "v_permlane16_swap_b32 v123, v127\n" \
  "v_exp_f32 v207, v207\n" \
  "v_exp_f32 v208, v208\n" \
  "v_exp_f32 v209, v209\n" \
  "v_smfmac_f32_32x32x32_bf16 v[232:247], v[120:123], v[200:207], v252\n" \
  "s_waitcnt vmcnt(6)\n" \
  "v_exp_f32 v210, v210\n" \
  "v_exp_f32 v211, v211\n" \
  "v_max_u32 v255, v40, v128\n" \
  "v_exp_f32 v212, v212\n" \
  "v_exp_f32 v213, v213\n" \
  "v_cmp_eq_u32 vcc, -1, v255\n" \
  "v_exp_f32 v214, v214\n" \
  "v_exp_f32 v215, v215\n" \
  "v_exp_f32 v216, v216\n" \
  "v_exp_f32 v217, v217\n" \
  "v_smfmac_f32_32x32x32_bf16 v[232:247], v[124:127], v[208:215], v252\n" \
  "s_cbranch_vccnz .Lrt7_%=\n" \
  ".Lbk7_%=:\n" \
  "buffer_load_dwordx4 v[56:59], v1, %[rs], %[kv3] offen offset:0 sc1\n" \
  "v_exp_f32 v218, v218\n" \
  "v_permlane16_swap_b32 v128, v132\n" \
  "v_exp_f32 v219, v219\n" \
  "v_permlane16_swap_b32 v129, v133\n" \
  "v_exp_f32 v220, v220\n" \
  "v_mfma_f32_32x32x16_bf16 v[200:215], v[40:43], v[248:251], 0\n" \
  "buffer_load_dwordx4 v[160:163], v2, %[rs], %[kv2] offen offset:3072 sc1\n" \
  "v_exp_f32 v221, v221\n" \
  "v_permlane16_swap_b32 v130, v134\n" \
  "v_exp_f32 v222, v222\n" \
  "v_permlane16_swap_b32 v131, v135\n" \
  "v_exp_f32 v223, v223\n" \
  "v_exp_f32 v224, v224\n" \
  "v_exp_f32 v225, v225\n" \
  "v_smfmac_f32_32x32x32_bf16 v[232:247], v[128:131], v[216:223], v252\n" \
  "s_waitcnt vmcnt(6)\n" \
  "v_exp_f32 v226, v226\n" \
  "v_exp_f32 v227, v227\n" \
  "v_max_u32 v255, v44, v136\n" \
  "v_exp_f32 v228, v228\n" \
  "v_exp_f32 v229, v229\n" \
  "v_cmp_eq_u32 vcc, -1, v255\n" \
  "v_exp_f32 v230, v230\n" \
  "v_exp_f32 v231, v231\n" \
  "v_exp_f32 v200, v200\n" \
  "v_exp_f32 v201, v201\n" \
  "v_smfmac_f32_32x32x32_bf16 v[232:247], v[132:135], v[224:231], v252\n" \
  "s_cbranch_vccnz .Lrt8_%=\n" \
  ".Lbk8_%=:\n" \
  "buffer_load_dwordx4 v[60:63], v1, %[rs], %[kv3] offen offset:1024 sc1\n" \
  "v_exp_f32 v202, v202\n" \
  "v_permlane16_swap_b32 v136, v140\n" \
  "v_exp_f32 v203, v203\n" \
  "v_permlane16_swap_b32 v137, v141\n" \
  "v_exp_f32 v204, v204\n" \
  "v_mfma_f32_32x32x16_bf16 v[216:231], v[44:47], v[248:251], 0\n" \
  "buffer_load_dwordx4 v[168:171], v2, %[rs], %[kv3] offen offset:0 sc1\n" \
  "v_exp_f32 v205, v205\n" \
  "v_permlane16_swap_b32 v138, v142\n" \
  "v_exp_f32 v206, v206\n" \
  "v_permlane16_swap_b32 v139, v143\n" \
  "v_exp_f32 v207, v207\n" \
  "v_exp_f32 v208, v208\n" \
  "v_exp_f32 v209, v209\n" \
  "v_smfmac_f32_32x32x32_bf16 v[232:247], v[136:139], v[200:207], v252\n" \
  "s_waitcnt vmcnt(6)\n" \
  "v_exp_f32 v210, v210\n" \
  "v_exp_f32 v211, v211\n" \
  "v_max_u32 v255, v48, v144\n" \
  "v_exp_f32 v212, v212\n" \
  "v_exp_f32 v213, v213\n" \
  "v_cmp_eq_u32 vcc, -1, v255\n" \
  "v_exp_f32 v214, v214\n" \
  "v_exp_f32 v215, v215\n" \
  "v_exp_f32 v216, v216\n" \
  "v_exp_f32 v217, v217\n" \
  "v_smfmac_f32_32x32x32_bf16 v[232:247], v[140:143], v[208:215], v252\n" \
  "s_cbranch_vccnz .Lrt9_%=\n" \
  ".Lbk9_%=:\n" \
  "buffer_load_dwordx4 v[64:67], v1, %[rs], %[kv3] offen offset:2048 sc1\n" \
  "v_exp_f32 v218, v218\n" \
  "v_permlane16_swap_b32 v144, v148\n" \
  "v_exp_f32 v219, v219\n" \
  "v_permlane16_swap_b32 v145, v149\n" \
  "v_exp_f32 v220, v220\n" \
  "v_mfma_f32_32x32x16_bf16 v[200:215], v[48:51], v[248:251], 0\n" \
  "buffer_load_dwordx4 v[176:179], v2, %[rs], %[kv3] offen offset:1024 sc1\n" \
  "v_exp_f32 v221, v221\n" \
  "v_permlane16_swap_b32 v146, v150\n" \
  "v_exp_f32 v222, v222\n" \
  "v_permlane16_swap_b32 v147, v151\n" \
  "v_exp_f32 v223, v223\n" \
  "v_exp_f32 v224, v224\n" \
  "v_exp_f32 v225, v225\n" \
  "v_smfmac_f32_32x32x32_bf16 v[232:247], v[144:147], v[216:223], v252\n" \
  "s_waitcnt vmcnt(6)\n" \
  "v_exp_f32 v226, v226\n" \
  "v_exp_f32 v227, v227\n" \
  "v_max_u32 v255, v52, v152\n" \
  "v_exp_f32 v228, v228\n" \
  "v_exp_f32 v229, v229\n" \
  "v_cmp_eq_u32 vcc, -1, v255\n" \
  "v_exp_f32 v230, v230\n" \
  "v_exp_f32 v231, v231\n" \
  "v_exp_f32 v200, v200\n" \
  "v_exp_f32 v201, v201\n" \
  "v_smfmac_f32_32x32x32_bf16 v[232:247], v[148:151], v[224:231], v252\n" \
  "s_cbranch_vccnz .Lrt10_%=\n" \
  ".Lbk10_%=:\n" \
  "buffer_load_dwordx4 v[68:71], v1, %[rs], %[kv3] offen offset:3072 sc1\n" \
  "v_exp_f32 v202, v202\n" \
  "v_permlane16_swap_b32 v152, v156\n" \
  "v_exp_f32 v203, v203\n" \
  "v_permlane16_swap_b32 v153, v157\n" \
  "v_exp_f32 v204, v204\n" \
  "v_mfma_f32_32x32x16_bf16 v[216:231], v[52:55], v[248:251], 0\n" \
  "buffer_load_dwordx4 v[184:187], v2, %[rs], %[kv3] offen offset:2048 sc1\n" \
  "v_exp_f32 v205, v205\n" \
  "v_permlane16_swap_b32 v154, v158\n" \
  "v_exp_f32 v206, v206\n" \
  "v_permlane16_swap_b32 v155, v159\n" \
  "v_exp_f32 v207, v207\n" \
  "v_exp_f32 v208, v208\n" \
  "v_exp_f32 v209, v209\n" \
  "v_smfmac_f32_32x32x32_bf16 v[232:247], v[152:155], v[200:207], v252\n" \
  "s_waitcnt vmcnt(6)\n" \
  "v_exp_f32 v210, v210\n" \
  "v_exp_f32 v211, v211\n" \
  "v_max_u32 v255, v56, v160\n" \
  "v_exp_f32 v212, v212\n" \
  "v_exp_f32 v213, v213\n" \
  "v_cmp_eq_u32 vcc, -1, v255\n" \
  "v_exp_f32 v214, v214\n" \
  "v_exp_f32 v215, v215\n" \
  "v_exp_f32 v216, v216\n" \
  "v_exp_f32 v217, v217\n" \
  "v_smfmac_f32_32x32x32_bf16 v[232:247], v[156:159], v[208:215], v252\n" \
  "s_cbranch_vccnz .Lrt11_%=\n" \
  ".Lbk11_%=:\n" \
  "buffer_load_dwordx4 v[192:195], v2, %[rs], %[kv3] offen offset:3072 sc1\n" \
  "v_exp_f32 v218, v218\n" \
  "v_permlane16_swap_b32 v160, v164\n" \
  "v_exp_f32 v219, v219\n" \
  "v_permlane16_swap_b32 v161, v165\n" \
  "v_exp_f32 v220, v220\n" \
  "v_mfma_f32_32x32x16_bf16 v[200:215], v[56:59], v[248:251], 0\n" \
  "v_exp_f32 v221, v221\n" \
  "v_permlane16_swap_b32 v162, v166\n" \
  "v_exp_f32 v222, v222\n" \
  "v_permlane16_swap_b32 v163, v167\n" \
  "v_exp_f32 v223, v223\n" \
  "v_exp_f32 v224, v224\n" \
  "v_exp_f32 v225, v225\n" \
  "v_smfmac_f32_32x32x32_bf16 v[232:247], v[160:163], v[216:223], v252\n" \
  "s_waitcnt vmcnt(5)\n" \
  "s_waitcnt lgkmcnt(0)\n" \
  "s_barrier\n" \
  "v_exp_f32 v226, v226\n" \
  "v_exp_f32 v227, v227\n" \
  "v_max_u32 v255, v60, v168\n" \
  "v_exp_f32 v228, v228\n" \
  "v_exp_f32 v229, v229\n" \
  "v_cmp_eq_u32 vcc, -1, v255\n" \
  "v_exp_f32 v230, v230\n" \
  "v_exp_f32 v231, v231\n" \
  "v_exp_f32 v200, v200\n" \
  "v_exp_f32 v201, v201\n" \
  "v_smfmac_f32_32x32x32_bf16 v[232:247], v[164:167], v[224:231], v252\n" \
  "s_cbranch_vccnz .Lrt12_%=\n" \
  ".Lbk12_%=:\n" \
  "v_exp_f32 v202, v202\n" \
  "v_permlane16_swap_b32 v168, v172\n" \
  "v_exp_f32 v203, v203\n" \
  "v_permlane16_swap_b32 v169, v173\n" \
  "v_exp_f32 v204, v204\n" \
  "v_mfma_f32_32x32x16_bf16 v[216:231], v[60:63], v[248:251], 0\n" \
  "v_exp_f32 v205, v205\n" \
  "v_permlane16_swap_b32 v170, v174\n" \
  "v_exp_f32 v206, v206\n" \
  "v_permlane16_swap_b32 v171, v175\n" \
  "v_exp_f32 v207, v207\n" \
  "v_exp_f32 v208, v208\n" \
  "v_exp_f32 v209, v209\n" \
  "v_smfmac_f32_32x32x32_bf16 v[232:247], v[168:171], v[200:207], v252\n" \
  "s_waitcnt vmcnt(3)\n" \
  "v_exp_f32 v210, v210\n" \
  "v_exp_f32 v211, v211\n" \
  "v_max_u32 v255, v64, v176\n" \
  "v_exp_f32 v212, v212\n" \
  "v_exp_f32 v213, v213\n" \
  "v_cmp_eq_u32 vcc, -1, v255\n" \
  "v_exp_f32 v214, v214\n" \
  "v_exp_f32 v215, v215\n" \
  "v_exp_f32 v216, v216\n" \
  "v_exp_f32 v217, v217\n" \
  "v_smfmac_f32_32x32x32_bf16 v[232:247], v[172:175], v[208:215], v252\n" \
  "s_cbranch_vccnz .Lrt13_%=\n" \
  ".Lbk13_%=:\n" \
  "v_exp_f32 v218, v218\n" \
  "v_permlane16_swap_b32 v176, v180\n" \
  "v_exp_f32 v219, v219\n" \
  "v_permlane16_swap_b32 v177, v181\n" \
  "v_exp_f32 v220, v220\n" \
  "v_mfma_f32_32x32x16_bf16 v[200:215], v[64:67], v[248:251], 0\n" \
  "v_exp_f32 v221, v221\n" \
  "v_permlane16_swap_b32 v178, v182\n" \
  "v_exp_f32 v222, v222\n" \
  "v_permlane16_swap_b32 v179, v183\n" \
  "v_exp_f32 v223, v223\n" \
  "v_exp_f32 v224, v224\n" \
  "v_exp_f32 v225, v225\n" \
  "v_smfmac_f32_32x32x32_bf16 v[232:247], v[176:179], v[216:223], v252\n" \
  "s_waitcnt vmcnt(1)\n" \
  "v_exp_f32 v226, v226\n" \
  "v_exp_f32 v227, v227\n" \
  "v_max_u32 v255, v68, v184\n" \
  "v_exp_f32 v228, v228\n" \
  "v_exp_f32 v229, v229\n" \
  "v_cmp_eq_u32 vcc, -1, v255\n" \
  "v_exp_f32 v230, v230\n" \
  "v_exp_f32 v231, v231\n" \
  "v_exp_f32 v200, v200\n" \
  "v_exp_f32 v201, v201\n" \
  "v_smfmac_f32_32x32x32_bf16 v[232:247], v[180:183], v[224:231], v252\n" \
  "s_cbranch_vccnz .Lrt14_%=\n" \
  ".Lbk14_%=:\n" \
  "v_exp_f32 v202, v202\n" \
  "v_permlane16_swap_b32 v184, v188\n" \
  "v_exp_f32 v203, v203\n" \
  "v_permlane16_swap_b32 v185, v189\n" \
  "v_exp_f32 v204, v204\n" \
  "v_mfma_f32_32x32x16_bf16 v[216:231], v[68:71], v[248:251], 0\n" \
  "ds_read_b128 v[248:251], v253 offset:9216\n" \
  "v_exp_f32 v205, v205\n" \
  "v_permlane16_swap_b32 v186, v190\n" \
  "v_exp_f32 v206, v206\n" \
  "v_permlane16_swap_b32 v187, v191\n" \
  "v_exp_f32 v207, v207\n" \
  "v_exp_f32 v208, v208\n" \
  "v_exp_f32 v209, v209\n" \
  "v_smfmac_f32_32x32x32_bf16 v[232:247], v[184:187], v[200:207], v252\n" \
  "s_waitcnt vmcnt(0)\n" \
  "v_exp_f32 v210, v210\n" \
  "v_exp_f32 v211, v211\n" \
  "v_cmp_eq_u32 vcc, -1, v192\n" \
  "v_exp_f32 v212, v212\n" \
  "v_exp_f32 v213, v213\n" \
  "v_exp_f32 v214, v214\n" \
  "v_exp_f32 v215, v215\n" \
  "v_exp_f32 v216, v216\n" \
  "v_exp_f32 v217, v217\n" \
  "v_smfmac_f32_32x32x32_bf16 v[232:247], v[188:191], v[208:215], v252\n" \
  "s_cbranch_vccnz .Lrt15_%=\n" \
  ".Lbk15_%=:\n" \
  "s_waitcnt lgkmcnt(0)\n" \
  "v_exp_f32 v218, v218\n" \
  "v_permlane16_swap_b32 v192, v196\n" \
  "v_exp_f32 v219, v219\n" \
  "v_permlane16_swap_b32 v193, v197\n" \
  "v_exp_f32 v220, v220\n" \
  "v_mfma_f32_32x32x16_bf16 v[200:215], v[8:11], v[248:251], 0\n" \
  "v_exp_f32 v221, v221\n" \
  "v_permlane16_swap_b32 v194, v198\n" \
  "v_exp_f32 v222, v222\n" \
  "v_permlane16_swap_b32 v195, v199\n" \
  "v_exp_f32 v223, v223\n" \
  "v_exp_f32 v224, v224\n" \
  "v_exp_f32 v225, v225\n" \
  "v_smfmac_f32_32x32x32_bf16 v[232:247], v[192:195], v[216:223], v252\n" \
  "v_exp_f32 v226, v226\n" \
  "v_exp_f32 v227, v227\n" \
  "v_exp_f32 v228, v228\n" \
  "v_exp_f32 v229, v229\n" \
  "v_exp_f32 v230, v230\n" \
  "v_exp_f32 v231, v231\n" \
  "v_exp_f32 v200, v200\n" \
  "v_exp_f32 v201, v201\n" \
  "v_smfmac_f32_32x32x32_bf16 v[232:247], v[196:199], v[224:231], v252\n" \
  "v_exp_f32 v202, v202\n" \
  "v_exp_f32 v203, v203\n" \
  "v_exp_f32 v204, v204\n" \
  "v_mfma_f32_32x32x16_bf16 v[216:231], v[12:15], v[248:251], 0\n" \
  "v_exp_f32 v205, v205\n" \
  "v_exp_f32 v206, v206\n" \
  "v_exp_f32 v207, v207\n" \
  "v_exp_f32 v208, v208\n" \
  "v_exp_f32 v209, v209\n" \
  "v_exp_f32 v210, v210\n" \
  "v_exp_f32 v211, v211\n" \
  "v_exp_f32 v212, v212\n" \
  "v_mov_b64 v[0:1], v[232:233]\n" \
  "v_mov_b64 v[2:3], v[234:235]\n" \
  "v_mov_b64 v[4:5], v[236:237]\n" \
  "v_mov_b64 v[6:7], v[238:239]\n" \
  "v_mov_b32 v255, v240\n" \
  "v_mov_b64 v[232:233], 0\n" \
  "v_mov_b64 v[234:235], 0\n" \
  "v_mov_b64 v[236:237], 0\n" \
  "v_mov_b64 v[238:239], 0\n" \
  "v_mov_b64 v[240:241], 0\n" \
  "v_exp_f32 v213, v213\n" \
  "v_exp_f32 v214, v214\n" \
  "v_smfmac_f32_32x32x32_bf16 v[232:247], v[72:75], v[200:207], v252\n" \
  "v_exp_f32 v215, v215\n" \
  "v_exp_f32 v216, v216\n" \
  "v_exp_f32 v217, v217\n" \
  "v_smfmac_f32_32x32x32_bf16 v[232:247], v[76:79], v[208:215], v252\n" \
  "ds_write_b128 v254, v[0:3] offset:16384\n" \
  "v_exp_f32 v218, v218\n" \
  "v_exp_f32 v219, v219\n" \
  "v_exp_f32 v220, v220\n" \
  "v_mfma_f32_32x32x16_bf16 v[200:215], v[16:19], v[248:251], 0\n" \
  "v_exp_f32 v221, v221\n" \
  "v_exp_f32 v222, v222\n" \
  "v_exp_f32 v223, v223\n" \
  "v_exp_f32 v224, v224\n" \
  "v_exp_f32 v225, v225\n" \
  "v_smfmac_f32_32x32x32_bf16 v[232:247], v[80:83], v[216:223], v252\n" \
  "ds_write_b128 v254, v[4:7] offset:17408\n" \
  "v_exp_f32 v226, v226\n" \
  "v_exp_f32 v227, v227\n" \
  "v_exp_f32 v228, v228\n" \
  "v_exp_f32 v229, v229\n" \
  "v_exp_f32 v230, v230\n" \
  "v_exp_f32 v231, v231\n" \
  "v_exp_f32 v200, v200\n" \
  "v_exp_f32 v201, v201\n" \
  "v_smfmac_f32_32x32x32_bf16 v[232:247], v[84:87], v[224:231], v252\n" \
  "ds_write_b32 v254, v255 offset:18432\n" \
  "v_exp_f32 v202, v202\n" \
  "v_exp_f32 v203, v203\n" \
  "v_exp_f32 v204, v204\n" \
  "v_mfma_f32_32x32x16_bf16 v[216:231], v[20:23], v[248:251], 0\n" \
  "v_exp_f32 v205, v205\n" \
  "v_exp_f32 v206, v206\n" \
  "v_exp_f32 v207, v207\n" \
  "v_exp_f32 v208, v208\n" \
  "v_exp_f32 v209, v209\n" \
  "v_smfmac_f32_32x32x32_bf16 v[232:247], v[88:91], v[200:207], v252\n" \
  "v_exp_f32 v210, v210\n" \
  "v_exp_f32 v211, v211\n" \
  "v_exp_f32 v212, v212\n" \
  "v_exp_f32 v213, v213\n" \
  "v_exp_f32 v214, v214\n" \
  "v_exp_f32 v215, v215\n" \
  "v_exp_f32 v216, v216\n" \
  "v_exp_f32 v217, v217\n" \
  "v_smfmac_f32_32x32x32_bf16 v[232:247], v[92:95], v[208:215], v252\n" \
  "v_exp_f32 v218, v218\n" \
  "v_exp_f32 v219, v219\n" \
  "v_exp_f32 v220, v220\n" \
  "v_mfma_f32_32x32x16_bf16 v[200:215], v[24:27], v[248:251], 0\n" \
  "v_exp_f32 v221, v221\n" \
  "v_exp_f32 v222, v222\n" \
  "v_exp_f32 v223, v223\n" \
  "v_exp_f32 v224, v224\n" \
  "v_exp_f32 v225, v225\n" \
  "v_smfmac_f32_32x32x32_bf16 v[232:247], v[96:99], v[216:223], v252\n" \
  "v_exp_f32 v226, v226\n" \
  "v_exp_f32 v227, v227\n" \
  "v_exp_f32 v228, v228\n" \
  "v_exp_f32 v229, v229\n" \
  "v_exp_f32 v230, v230\n" \
  "v_exp_f32 v231, v231\n" \
  "v_exp_f32 v200, v200\n" \
  "v_exp_f32 v201, v201\n" \
  "v_smfmac_f32_32x32x32_bf16 v[232:247], v[100:103], v[224:231], v252\n" \
  "v_exp_f32 v202, v202\n" \
  "v_exp_f32 v203, v203\n" \
  "v_exp_f32 v204, v204\n" \
  "v_mfma_f32_32x32x16_bf16 v[216:231], v[28:31], v[248:251], 0\n" \
  "v_exp_f32 v205, v205\n" \
  "v_exp_f32 v206, v206\n" \
  "v_exp_f32 v207, v207\n" \
  "v_exp_f32 v208, v208\n" \
  "v_exp_f32 v209, v209\n" \
  "v_smfmac_f32_32x32x32_bf16 v[232:247], v[104:107], v[200:207], v252\n" \
  "v_exp_f32 v210, v210\n" \
  "v_exp_f32 v211, v211\n" \
  "v_exp_f32 v212, v212\n" \
  "v_exp_f32 v213, v213\n" \
  "v_exp_f32 v214, v214\n" \
  "v_exp_f32 v215, v215\n" \
  "v_exp_f32 v216, v216\n" \
  "v_exp_f32 v217, v217\n" \
  "v_smfmac_f32_32x32x32_bf16 v[232:247], v[108:111], v[208:215], v252\n" \
  "v_exp_f32 v218, v218\n" \
  "v_exp_f32 v219, v219\n" \
  "v_exp_f32 v220, v220\n" \
  "v_mfma_f32_32x32x16_bf16 v[200:215], v[32:35], v[248:251], 0\n" \
  "v_exp_f32 v221, v221\n" \
  "v_exp_f32 v222, v222\n" \
  "v_exp_f32 v223, v223\n" \
  "v_exp_f32 v224, v224\n" \
  "v_exp_f32 v225, v225\n" \
  "v_smfmac_f32_32x32x32_bf16 v[232:247], v[112:115], v[216:223], v252\n" \
  "v_exp_f32 v226, v226\n" \
  "v_exp_f32 v227, v227\n" \
  "v_exp_f32 v228, v228\n" \
  "v_exp_f32 v229, v229\n" \
  "v_exp_f32 v230, v230\n" \
  "v_exp_f32 v231, v231\n" \
  "v_exp_f32 v200, v200\n" \
  "v_exp_f32 v201, v201\n" \
  "v_smfmac_f32_32x32x32_bf16 v[232:247], v[116:119], v[224:231], v252\n" \
  "v_exp_f32 v202, v202\n" \
  "v_exp_f32 v203, v203\n" \
  "v_exp_f32 v204, v204\n" \
  "v_mfma_f32_32x32x16_bf16 v[216:231], v[36:39], v[248:251], 0\n" \
  "v_exp_f32 v205, v205\n" \
  "v_exp_f32 v206, v206\n" \
  "v_exp_f32 v207, v207\n" \
  "v_exp_f32 v208, v208\n" \
  "v_exp_f32 v209, v209\n" \
  "v_smfmac_f32_32x32x32_bf16 v[232:247], v[120:123], v[200:207], v252\n" \
  "v_exp_f32 v210, v210\n" \
  "v_exp_f32 v211, v211\n" \
  "v_exp_f32 v212, v212\n" \
  "v_exp_f32 v213, v213\n" \
  "v_exp_f32 v214, v214\n" \
  "v_exp_f32 v215, v215\n" \
  "v_exp_f32 v216, v216\n" \
  "v_exp_f32 v217, v217\n" \
  "v_smfmac_f32_32x32x32_bf16 v[232:247], v[124:127], v[208:215], v252\n" \
  "v_exp_f32 v218, v218\n" \
  "v_exp_f32 v219, v219\n" \
  "v_exp_f32 v220, v220\n" \
  "v_mfma_f32_32x32x16_bf16 v[200:215], v[40:43], v[248:251], 0\n" \
  "v_exp_f32 v221, v221\n" \
  "v_exp_f32 v222, v222\n" \
  "v_exp_f32 v223, v223\n" \
  "v_exp_f32 v224, v224\n" \
  "v_exp_f32 v225, v225\n" \
  "v_smfmac_f32_32x32x32_bf16 v[232:247], v[128:131], v[216:223], v252\n" \
  "v_exp_f32 v226, v226\n" \
  "v_exp_f32 v227, v227\n" \
  "v_exp_f32 v228, v228\n" \
  "v_exp_f32 v229, v229\n" \
  "v_exp_f32 v230, v230\n" \
  "v_exp_f32 v231, v231\n" \
  "v_exp_f32 v200, v200\n" \
  "v_exp_f32 v201, v201\n" \
  "v_smfmac_f32_32x32x32_bf16 v[232:247], v[132:135], v[224:231], v252\n" \
  "v_exp_f32 v202, v202\n" \
  "v_exp_f32 v203, v203\n" \
  "v_exp_f32 v204, v204\n" \
  "v_mfma_f32_32x32x16_bf16 v[216:231], v[44:47], v[248:251], 0\n" \
  "v_exp_f32 v205, v205\n" \
  "v_exp_f32 v206, v206\n" \
  "v_exp_f32 v207, v207\n" \
  "v_exp_f32 v208, v208\n" \
  "v_exp_f32 v209, v209\n" \
  "v_smfmac_f32_32x32x32_bf16 v[232:247], v[136:139], v[200:207], v252\n" \
  "v_exp_f32 v210, v210\n" \
  "v_exp_f32 v211, v211\n" \
  "v_exp_f32 v212, v212\n" \
  "v_exp_f32 v213, v213\n" \
  "v_exp_f32 v214, v214\n" \
  "v_exp_f32 v215, v215\n" \
  "v_exp_f32 v216, v216\n" \
  "v_exp_f32 v217, v217\n" \
  "v_smfmac_f32_32x32x32_bf16 v[232:247], v[140:143], v[208:215], v252\n" \
  "v_exp_f32 v218, v218\n" \
  "v_exp_f32 v219, v219\n" \
  "v_exp_f32 v220, v220\n" \
  "v_mfma_f32_32x32x16_bf16 v[200:215], v[48:51], v[248:251], 0\n" \
  "v_exp_f32 v221, v221\n" \
  "v_exp_f32 v222, v222\n" \
  "v_exp_f32 v223, v223\n" \
  "v_exp_f32 v224, v224\n" \
  "v_exp_f32 v225, v225\n" \
  "v_smfmac_f32_32x32x32_bf16 v[232:247], v[144:147], v[216:223], v252\n" \
  "v_exp_f32 v226, v226\n" \
  "v_exp_f32 v227, v227\n" \
  "v_exp_f32 v228, v228\n" \
  "v_exp_f32 v229, v229\n" \
  "v_exp_f32 v230, v230\n" \
  "v_exp_f32 v231, v231\n" \
  "v_exp_f32 v200, v200\n" \
  "v_exp_f32 v201, v201\n" \
  "v_smfmac_f32_32x32x32_bf16 v[232:247], v[148:151], v[224:231], v252\n" \
  "v_exp_f32 v202, v202\n" \
  "v_exp_f32 v203, v203\n" \
  "v_exp_f32 v204, v204\n" \
  "v_mfma_f32_32x32x16_bf16 v[216:231], v[52:55], v[248:251], 0\n" \
  "v_exp_f32 v205, v205\n" \
  "v_exp_f32 v206, v206\n" \
  "v_exp_f32 v207, v207\n" \
  "v_exp_f32 v208, v208\n" \
  "v_exp_f32 v209, v209\n" \
  "v_smfmac_f32_32x32x32_bf16 v[232:247], v[152:155], v[200:207], v252\n" \
  "v_exp_f32 v210, v210\n" \
  "v_exp_f32 v211, v211\n" \
  "v_exp_f32 v212, v212\n" \
  "v_exp_f32 v213, v213\n" \
  "v_exp_f32 v214, v214\n" \
  "v_exp_f32 v215, v215\n" \
  "v_exp_f32 v216, v216\n" \
  "v_exp_f32 v217, v217\n" \
  "v_smfmac_f32_32x32x32_bf16 v[232:247], v[156:159], v[208:215], v252\n" \
  "v_exp_f32 v218, v218\n" \
  "v_exp_f32 v219, v219\n" \
  "v_exp_f32 v220, v220\n" \
  "v_mfma_f32_32x32x16_bf16 v[200:215], v[56:59], v[248:251], 0\n" \
  "v_exp_f32 v221, v221\n" \
  "v_exp_f32 v222, v222\n" \
  "v_exp_f32 v223, v223\n" \
  "v_exp_f32 v224, v224\n" \
  "v_exp_f32 v225, v225\n" \
  "v_smfmac_f32_32x32x32_bf16 v[232:247], v[160:163], v[216:223], v252\n" \
  "v_exp_f32 v226, v226\n" \
  "v_exp_f32 v227, v227\n" \
  "v_exp_f32 v228, v228\n" \
  "v_exp_f32 v229, v229\n" \
  "v_exp_f32 v230, v230\n" \
  "v_exp_f32 v231, v231\n" \
  "v_exp_f32 v200, v200\n" \
  "v_exp_f32 v201, v201\n" \
  "v_smfmac_f32_32x32x32_bf16 v[232:247], v[164:167], v[224:231], v252\n" \
  "v_exp_f32 v202, v202\n" \
  "v_exp_f32 v203, v203\n" \
  "v_exp_f32 v204, v204\n" \
  "v_mfma_f32_32x32x16_bf16 v[216:231], v[60:63], v[248:251], 0\n" \
  "v_exp_f32 v205, v205\n" \
  "v_exp_f32 v206, v206\n" \
  "v_exp_f32 v207, v207\n" \
  "v_exp_f32 v208, v208\n" \
  "v_exp_f32 v209, v209\n" \
  "v_smfmac_f32_32x32x32_bf16 v[232:247], v[168:171], v[200:207], v252\n" \
  "v_exp_f32 v210, v210\n" \
  "v_exp_f32 v211, v211\n" \
  "v_exp_f32 v212, v212\n" \
  "v_exp_f32 v213, v213\n" \
  "v_exp_f32 v214, v214\n" \
  "v_exp_f32 v215, v215\n" \
  "v_exp_f32 v216, v216\n" \
  "v_exp_f32 v217, v217\n" \
  "v_smfmac_f32_32x32x32_bf16 v[232:247], v[172:175], v[208:215], v252\n" \
  "v_exp_f32 v218, v218\n" \
  "v_exp_f32 v219, v219\n" \
  "v_exp_f32 v220, v220\n" \
  "v_mfma_f32_32x32x16_bf16 v[200:215], v[64:67], v[248:251], 0\n" \
  "v_exp_f32 v221, v221\n" \
  "v_exp_f32 v222, v222\n" \
  "v_exp_f32 v223, v223\n" \
  "v_exp_f32 v224, v224\n" \
  "v_exp_f32 v225, v225\n" \
  "v_smfmac_f32_32x32x32_bf16 v[232:247], v[176:179], v[216:223], v252\n" \
  "s_waitcnt lgkmcnt(0)\n" \
  "s_barrier\n" \
  "v_exp_f32 v226, v226\n" \
  "v_exp_f32 v227, v227\n" \
  "v_exp_f32 v228, v228\n" \
  "v_exp_f32 v229, v229\n" \
  "v_exp_f32 v230, v230\n" \
  "v_exp_f32 v231, v231\n" \
  "v_exp_f32 v200, v200\n" \
  "v_exp_f32 v201, v201\n" \
  "v_smfmac_f32_32x32x32_bf16 v[232:247], v[180:183], v[224:231], v252\n" \
  "v_exp_f32 v202, v202\n" \
  "v_exp_f32 v203, v203\n" \
  "v_exp_f32 v204, v204\n" \
  "v_mfma_f32_32x32x16_bf16 v[216:231], v[68:71], v[248:251], 0\n" \
  "ds_read_b128 v[248:251], v253 offset:10240\n" \
  "v_exp_f32 v205, v205\n" \
  "v_exp_f32 v206, v206\n" \
  "v_exp_f32 v207, v207\n" \
  "v_exp_f32 v208, v208\n" \
  "v_exp_f32 v209, v209\n" \
  "v_smfmac_f32_32x32x32_bf16 v[232:247], v[184:187], v[200:207], v252\n" \
  "v_exp_f32 v210, v210\n" \
  "v_exp_f32 v211, v211\n" \
  "v_exp_f32 v212, v212\n" \
  "v_exp_f32 v213, v213\n" \
  "v_exp_f32 v214, v214\n" \
  "v_exp_f32 v215, v215\n" \
  "v_exp_f32 v216, v216\n" \
  "v_exp_f32 v217, v217\n" \
  "v_smfmac_f32_32x32x32_bf16 v[232:247], v[188:191], v[208:215], v252\n" \
  "s_waitcnt lgkmcnt(0)\n" \
  "v_exp_f32 v218, v218\n" \
  "v_exp_f32 v219, v219\n" \
  "v_exp_f32 v220, v220\n" \
  "v_mfma_f32_32x32x16_bf16 v[200:215], v[8:11], v[248:251], 0\n" \
  "v_exp_f32 v221, v221\n" \
  "v_exp_f32 v222, v222\n" \
  "v_exp_f32 v223, v223\n" \
  "v_exp_f32 v224, v224\n" \
  "v_exp_f32 v225, v225\n" \
  "v_smfmac_f32_32x32x32_bf16 v[232:247], v[192:195], v[216:223], v252\n" \
  "v_exp_f32 v226, v226\n" \
  "v_exp_f32 v227, v227\n" \
  "v_exp_f32 v228, v228\n" \
  "v_exp_f32 v229, v229\n" \
  "v_exp_f32 v230, v230\n" \
  "v_exp_f32 v231, v231\n" \
  "v_exp_f32 v200, v200\n" \
  "v_exp_f32 v201, v201\n" \
  "v_smfmac_f32_32x32x32_bf16 v[232:247], v[196:199], v[224:231], v252\n" \
  "v_exp_f32 v202, v202\n" \
  "v_exp_f32 v203, v203\n" \
  "v_exp_f32 v204, v204\n" \
  "v_mfma_f32_32x32x16_bf16 v[216:231], v[12:15], v[248:251], 0\n" \
  "v_exp_f32 v205, v205\n" \
  "v_exp_f32 v206, v206\n" \
  "v_exp_f32 v207, v207\n" \
  "v_exp_f32 v208, v208\n" \
  "v_exp_f32 v209, v209\n" \
  "v_exp_f32 v210, v210\n" \
  "v_exp_f32 v211, v211\n" \
  "v_exp_f32 v212, v212\n" \
  "v_mov_b64 v[0:1], v[232:233]\n" \
  "v_mov_b64 v[2:3], v[234:235]\n" \
  "v_mov_b64 v[4:5], v[236:237]\n" \
  "v_mov_b64 v[6:7], v[238:239]\n" \
  "v_mov_b32 v255, v240\n" \
  "v_mov_b64 v[232:233], 0\n" \
  "v_mov_b64 v[234:235], 0\n" \
  "v_mov_b64 v[236:237], 0\n" \
  "v_mov_b64 v[238:239], 0\n" \
  "v_mov_b64 v[240:241], 0\n" \
  "v_exp_f32 v213, v213\n" \
  "v_exp_f32 v214, v214\n" \
  "v_smfmac_f32_32x32x32_bf16 v[232:247], v[72:75], v[200:207], v252\n" \
  "v_exp_f32 v215, v215\n" \
  "v_exp_f32 v216, v216\n" \
  "v_exp_f32 v217, v217\n" \
  "v_smfmac_f32_32x32x32_bf16 v[232:247], v[76:79], v[208:215], v252\n" \
  "ds_write_b128 v254, v[0:3] offset:28672\n" \
  "v_exp_f32 v218, v218\n" \
  "v_exp_f32 v219, v219\n" \
  "v_exp_f32 v220, v220\n" \
  "v_mfma_f32_32x32x16_bf16 v[200:215], v[16:19], v[248:251], 0\n" \
  "v_exp_f32 v221, v221\n" \
  "v_exp_f32 v222, v222\n" \
  "v_exp_f32 v223, v223\n" \
  "v_exp_f32 v224, v224\n" \
  "v_exp_f32 v225, v225\n" \
  "v_smfmac_f32_32x32x32_bf16 v[232:247], v[80:83], v[216:223], v252\n" \
  "ds_write_b128 v254, v[4:7] offset:29696\n" \
  "v_exp_f32 v226, v226\n" \
  "v_exp_f32 v227, v227\n" \
  "v_exp_f32 v228, v228\n" \
  "v_exp_f32 v229, v229\n" \
  "v_exp_f32 v230, v230\n" \
  "v_exp_f32 v231, v231\n" \
  "v_exp_f32 v200, v200\n" \
  "v_exp_f32 v201, v201\n" \
  "v_smfmac_f32_32x32x32_bf16 v[232:247], v[84:87], v[224:231], v252\n" \
  ".Lpad1_%=:\n" \
  "s_cbranch_execz .Lpad2_%=\n" \
  "ds_write_b32 v254, v255 offset:30720\n" \
  "v_exp_f32 v202, v202\n" \
  "v_exp_f32 v203, v203\n" \
  "v_exp_f32 v204, v204\n" \
  "v_mfma_f32_32x32x16_bf16 v[216:231], v[20:23], v[248:251], 0\n" \
  "v_exp_f32 v205, v205\n" \
  "v_exp_f32 v206, v206\n" \
  "v_exp_f32 v207, v207\n" \
  "v_exp_f32 v208, v208\n" \
  "v_exp_f32 v209, v209\n" \
  "v_smfmac_f32_32x32x32_bf16 v[232:247], v[88:91], v[200:207], v252\n" \
  "v_exp_f32 v210, v210\n" \
  "v_exp_f32 v211, v211\n" \
  "v_exp_f32 v212, v212\n" \
  "v_exp_f32 v213, v213\n" \
  "v_exp_f32 v214, v214\n" \
  "v_exp_f32 v215, v215\n" \
  "v_exp_f32 v216, v216\n" \
  "v_exp_f32 v217, v217\n" \
  "v_smfmac_f32_32x32x32_bf16 v[232:247], v[92:95], v[208:215], v252\n" \
  "v_exp_f32 v218, v218\n" \
  "v_exp_f32 v219, v219\n" \
  "v_exp_f32 v220, v220\n" \
  "v_mfma_f32_32x32x16_bf16 v[200:215], v[24:27], v[248:251], 0\n" \
  "v_exp_f32 v221, v221\n" \
  "v_exp_f32 v222, v222\n" \
  "v_exp_f32 v223, v223\n" \
  "v_exp_f32 v224, v224\n" \
  "v_exp_f32 v225, v225\n" \
  "v_smfmac_f32_32x32x32_bf16 v[232:247], v[96:99], v[216:223], v252\n" \
  "v_exp_f32 v226, v226\n" \
  "v_exp_f32 v227, v227\n" \
  "v_exp_f32 v228, v228\n" \
  "v_exp_f32 v229, v229\n" \
  "v_exp_f32 v230, v230\n" \
  "v_exp_f32 v231, v231\n" \
  "v_exp_f32 v200, v200\n" \
  "v_exp_f32 v201, v201\n" \
  "v_smfmac_f32_32x32x32_bf16 v[232:247], v[100:103], v[224:231], v252\n" \
  "v_exp_f32 v202, v202\n" \
  "v_exp_f32 v203, v203\n" \
  "v_exp_f32 v204, v204\n" \
  "v_mfma_f32_32x32x16_bf16 v[216:231], v[28:31], v[248:251], 0\n" \
  "v_exp_f32 v205, v205\n" \
  "v_exp_f32 v206, v206\n" \
  "v_exp_f32 v207, v207\n" \
  "v_exp_f32 v208, v208\n" \
  "v_exp_f32 v209, v209\n" \
  "v_smfmac_f32_32x32x32_bf16 v[232:247], v[104:107], v[200:207], v252\n" \
  "v_exp_f32 v210, v210\n" \
  "v_exp_f32 v211, v211\n" \
  "v_exp_f32 v212, v212\n" \
  "v_exp_f32 v213, v213\n" \
  "v_exp_f32 v214, v214\n" \
  "v_exp_f32 v215, v215\n" \
  "v_exp_f32 v216, v216\n" \
  "v_exp_f32 v217, v217\n" \
  "v_smfmac_f32_32x32x32_bf16 v[232:247], v[108:111], v[208:215], v252\n" \
  "v_exp_f32 v218, v218\n" \
  "v_exp_f32 v219, v219\n" \
  "v_exp_f32 v220, v220\n" \
  "v_mfma_f32_32x32x16_bf16 v[200:215], v[32:35], v[248:251], 0\n" \
  "v_exp_f32 v221, v221\n" \
  "v_exp_f32 v222, v222\n" \
  "v_exp_f32 v223, v223\n" \
  "v_exp_f32 v224, v224\n" \
  "v_exp_f32 v225, v225\n" \
  "v_smfmac_f32_32x32x32_bf16 v[232:247], v[112:115], v[216:223], v252\n" \
  "v_exp_f32 v226, v226\n" \
  "v_exp_f32 v227, v227\n" \
  "v_exp_f32 v228, v228\n" \
  "v_exp_f32 v229, v229\n" \
  "v_exp_f32 v230, v230\n" \
  "v_exp_f32 v231, v231\n" \
  "v_exp_f32 v200, v200\n" \
  "v_exp_f32 v201, v201\n" \
  "v_smfmac_f32_32x32x32_bf16 v[232:247], v[116:119], v[224:231], v252\n" \
  "v_exp_f32 v202, v202\n" \
  "v_exp_f32 v203, v203\n" \
  "v_exp_f32 v204, v204\n" \
  "v_mfma_f32_32x32x16_bf16 v[216:231], v[36:39], v[248:251], 0\n" \
  "v_exp_f32 v205, v205\n" \
  "v_exp_f32 v206, v206\n" \
  "v_exp_f32 v207, v207\n" \
  "v_exp_f32 v208, v208\n" \
  "v_exp_f32 v209, v209\n" \
  "v_smfmac_f32_32x32x32_bf16 v[232:247], v[120:123], v[200:207], v252\n" \
  "v_exp_f32 v210, v210\n" \
  "v_exp_f32 v211, v211\n" \
  "v_exp_f32 v212, v212\n" \
  "v_exp_f32 v213, v213\n" \
  "v_exp_f32 v214, v214\n" \
  "v_exp_f32 v215, v215\n" \
  "v_exp_f32 v216, v216\n" \
  "v_exp_f32 v217, v217\n" \
  "v_smfmac_f32_32x32x32_bf16 v[232:247], v[124:127], v[208:215], v252\n" \
  "v_exp_f32 v218, v218\n" \
  "v_exp_f32 v219, v219\n" \
  "v_exp_f32 v220, v220\n" \
  "v_mfma_f32_32x32x16_bf16 v[200:215], v[40:43], v[248:251], 0\n" \
  "v_exp_f32 v221, v221\n" \
  "v_exp_f32 v222, v222\n" \
  "v_exp_f32 v223, v223\n" \
  "v_exp_f32 v224, v224\n" \
  "v_exp_f32 v225, v225\n" \
  "v_smfmac_f32_32x32x32_bf16 v[232:247], v[128:131], v[216:223], v252\n" \
  "v_exp_f32 v226, v226\n" \
  "v_exp_f32 v227, v227\n" \
  "v_exp_f32 v228, v228\n" \
  "v_exp_f32 v229, v229\n" \
  "v_exp_f32 v230, v230\n" \
  "v_exp_f32 v231, v231\n" \
  "v_exp_f32 v200, v200\n" \
  "v_exp_f32 v201, v201\n" \
  "v_smfmac_f32_32x32x32_bf16 v[232:247], v[132:135], v[224:231], v252\n" \
  "v_exp_f32 v202, v202\n" \
  "v_exp_f32 v203, v203\n" \
  "v_exp_f32 v204, v204\n" \
  "v_mfma_f32_32x32x16_bf16 v[216:231], v[44:47], v[248:251], 0\n" \
  "v_exp_f32 v205, v205\n" \
  "v_exp_f32 v206, v206\n" \
  "v_exp_f32 v207, v207\n" \
  "v_exp_f32 v208, v208\n" \
  "v_exp_f32 v209, v209\n" \
  "v_smfmac_f32_32x32x32_bf16 v[232:247], v[136:139], v[200:207], v252\n" \
  "v_exp_f32 v210, v210\n" \
  "v_exp_f32 v211, v211\n" \
  "v_exp_f32 v212, v212\n" \
  "v_exp_f32 v213, v213\n" \
  "v_exp_f32 v214, v214\n" \
  "v_exp_f32 v215, v215\n" \
  "v_exp_f32 v216, v216\n" \
  "v_exp_f32 v217, v217\n" \
  "v_smfmac_f32_32x32x32_bf16 v[232:247], v[140:143], v[208:215], v252\n" \
  "v_exp_f32 v218, v218\n" \
  "v_exp_f32 v219, v219\n" \
  "v_exp_f32 v220, v220\n" \
  "v_mfma_f32_32x32x16_bf16 v[200:215], v[48:51], v[248:251], 0\n" \
  "v_exp_f32 v221, v221\n" \
  "v_exp_f32 v222, v222\n" \
  "v_exp_f32 v223, v223\n" \
  "v_exp_f32 v224, v224\n" \
  "v_exp_f32 v225, v225\n" \
  "v_smfmac_f32_32x32x32_bf16 v[232:247], v[144:147], v[216:223], v252\n" \
  "s_waitcnt lgkmcnt(0)\n" \
  "s_barrier\n" \
  "v_exp_f32 v226, v226\n" \
  "v_exp_f32 v227, v227\n" \
  "v_exp_f32 v228, v228\n" \
  "v_exp_f32 v229, v229\n" \
  "v_exp_f32 v230, v230\n" \
  "v_exp_f32 v231, v231\n" \
  "v_exp_f32 v200, v200\n" \
  "v_exp_f32 v201, v201\n" \
  "v_smfmac_f32_32x32x32_bf16 v[232:247], v[148:151], v[224:231], v252\n" \
  "v_exp_f32 v202, v202\n" \
  "v_exp_f32 v203, v203\n" \
  "v_exp_f32 v204, v204\n" \
  "v_mfma_f32_32x32x16_bf16 v[216:231], v[52:55], v[248:251], 0\n" \
  "v_exp_f32 v205, v205\n" \
  "v_exp_f32 v206, v206\n" \
  "v_exp_f32 v207, v207\n" \
  "v_exp_f32 v208, v208\n" \
  "v_exp_f32 v209, v209\n" \
  "v_smfmac_f32_32x32x32_bf16 v[232:247], v[152:155], v[200:207], v252\n" \
  "v_exp_f32 v210, v210\n" \
  "v_exp_f32 v211, v211\n" \
  "v_exp_f32 v212, v212\n" \
  "v_exp_f32 v213, v213\n" \
  "v_exp_f32 v214, v214\n" \
  "v_exp_f32 v215, v215\n" \
  "v_exp_f32 v216, v216\n" \
  "v_exp_f32 v217, v217\n" \
  "v_smfmac_f32_32x32x32_bf16 v[232:247], v[156:159], v[208:215], v252\n" \
  "v_exp_f32 v218, v218\n" \
  "v_exp_f32 v219, v219\n" \
  "v_exp_f32 v220, v220\n" \
  "v_mfma_f32_32x32x16_bf16 v[200:215], v[56:59], v[248:251], 0\n" \
  "v_exp_f32 v221, v221\n" \
  "v_exp_f32 v222, v222\n" \
  "v_exp_f32 v223, v223\n" \
  "v_exp_f32 v224, v224\n" \
  "v_exp_f32 v225, v225\n" \
  "v_smfmac_f32_32x32x32_bf16 v[232:247], v[160:163], v[216:223], v252\n" \
  "v_exp_f32 v226, v226\n" \
  "v_exp_f32 v227, v227\n" \
  "v_exp_f32 v228, v228\n" \
  "v_exp_f32 v229, v229\n" \
  "v_exp_f32 v230, v230\n" \
  "v_exp_f32 v231, v231\n" \
  "v_exp_f32 v200, v200\n" \
  "v_exp_f32 v201, v201\n" \
  "v_smfmac_f32_32x32x32_bf16 v[232:247], v[164:167], v[224:231], v252\n" \
  "v_exp_f32 v202, v202\n" \
  "v_exp_f32 v203, v203\n" \
  "v_exp_f32 v204, v204\n" \
  "v_mfma_f32_32x32x16_bf16 v[216:231], v[60:63], v[248:251], 0\n" \
  "v_exp_f32 v205, v205\n" \
  "v_exp_f32 v206, v206\n" \
  "v_exp_f32 v207, v207\n" \
  "v_exp_f32 v208, v208\n" \
  "v_exp_f32 v209, v209\n" \
  "v_smfmac_f32_32x32x32_bf16 v[232:247], v[168:171], v[200:207], v252\n" \
  "v_exp_f32 v210, v210\n" \
  "v_exp_f32 v211, v211\n" \
  "v_exp_f32 v212, v212\n" \
  "v_exp_f32 v213, v213\n" \
  "v_exp_f32 v214, v214\n" \
  "v_exp_f32 v215, v215\n" \
  "v_exp_f32 v216, v216\n" \
  "v_exp_f32 v217, v217\n" \
  "v_smfmac_f32_32x32x32_bf16 v[232:247], v[172:175], v[208:215], v252\n" \
  "v_exp_f32 v218, v218\n" \
  "v_exp_f32 v219, v219\n" \
  "v_exp_f32 v220, v220\n" \
  "v_mfma_f32_32x32x16_bf16 v[200:215], v[64:67], v[248:251], 0\n" \
  "v_exp_f32 v221, v221\n" \
  "v_exp_f32 v222, v222\n" \
  "v_exp_f32 v223, v223\n" \
  "v_exp_f32 v224, v224\n" \
  "v_exp_f32 v225, v225\n" \
  "v_smfmac_f32_32x32x32_bf16 v[232:247], v[176:179], v[216:223], v252\n" \
  "v_exp_f32 v226, v226\n" \
  "v_exp_f32 v227, v227\n" \
  "v_exp_f32 v228, v228\n" \
  "v_exp_f32 v229, v229\n" \
  "v_exp_f32 v230, v230\n" \
  "v_exp_f32 v231, v231\n" \
  "v_exp_f32 v200, v200\n" \
  "v_exp_f32 v201, v201\n" \
  "v_smfmac_f32_32x32x32_bf16 v[232:247], v[180:183], v[224:231], v252\n" \
  "v_exp_f32 v202, v202\n" \
  "v_exp_f32 v203, v203\n" \
  "v_exp_f32 v204, v204\n" \
  "v_mfma_f32_32x32x16_bf16 v[216:231], v[68:71], v[248:251], 0\n" \
  "ds_read_b128 v[248:251], v253 offset:11264\n" \
  "v_exp_f32 v205, v205\n" \
  "v_exp_f32 v206, v206\n" \
  "v_exp_f32 v207, v207\n" \
  "v_exp_f32 v208, v208\n" \
  "v_exp_f32 v209, v209\n" \
  "v_smfmac_f32_32x32x32_bf16 v[232:247], v[184:187], v[200:207], v252\n" \
  "v_exp_f32 v210, v210\n" \
  "v_exp_f32 v211, v211\n" \
  "v_exp_f32 v212, v212\n" \
  "v_exp_f32 v213, v213\n" \
  "v_exp_f32 v214, v214\n" \
  "v_exp_f32 v215, v215\n" \
  "v_exp_f32 v216, v216\n" \
  "v_exp_f32 v217, v217\n" \
  "v_smfmac_f32_32x32x32_bf16 v[232:247], v[188:191], v[208:215], v252\n" \
  "s_waitcnt lgkmcnt(0)\n" \
  "v_exp_f32 v218, v218\n" \
  "v_exp_f32 v219, v219\n" \
  "v_exp_f32 v220, v220\n" \
  "v_mfma_f32_32x32x16_bf16 v[200:215], v[8:11], v[248:251], 0\n" \
  "v_exp_f32 v221, v221\n" \
  "v_exp_f32 v222, v222\n" \
  "v_exp_f32 v223, v223\n" \
  "v_exp_f32 v224, v224\n" \
  "v_exp_f32 v225, v225\n" \
  "v_smfmac_f32_32x32x32_bf16 v[232:247], v[192:195], v[216:223], v252\n" \
  "v_exp_f32 v226, v226\n" \
  "v_exp_f32 v227, v227\n" \
  "v_exp_f32 v228, v228\n" \
  "v_exp_f32 v229, v229\n" \
  "v_exp_f32 v230, v230\n" \
  "v_exp_f32 v231, v231\n" \
  "v_exp_f32 v200, v200\n" \
  "v_exp_f32 v201, v201\n" \
  "v_smfmac_f32_32x32x32_bf16 v[232:247], v[196:199], v[224:231], v252\n" \
  "v_exp_f32 v202, v202\n" \
  "v_exp_f32 v203, v203\n" \
  "v_exp_f32 v204, v204\n" \
  "v_mfma_f32_32x32x16_bf16 v[216:231], v[12:15], v[248:251], 0\n" \
  "v_exp_f32 v205, v205\n" \
  "v_exp_f32 v206, v206\n" \
  "v_exp_f32 v207, v207\n" \
  "v_exp_f32 v208, v208\n" \
  "v_exp_f32 v209, v209\n" \
  "v_exp_f32 v210, v210\n" \
  "v_exp_f32 v211, v211\n" \
  "v_exp_f32 v212, v212\n" \
  "v_mov_b64 v[0:1], v[232:233]\n" \
  "v_mov_b64 v[2:3], v[234:235]\n" \
  "v_mov_b64 v[4:5], v[236:237]\n" \
  "v_mov_b64 v[6:7], v[238:239]\n" \
  "v_mov_b32 v255, v240\n" \
  "v_mov_b64 v[232:233], 0\n" \
  "v_mov_b64 v[234:235], 0\n" \
  "v_mov_b64 v[236:237], 0\n" \
  "v_mov_b64 v[238:239], 0\n" \
  "v_mov_b64 v[240:241], 0\n" \
  "v_exp_f32 v213, v213\n" \
  "v_exp_f32 v214, v214\n" \
  "v_smfmac_f32_32x32x32_bf16 v[232:247], v[72:75], v[200:207], v252\n" \
  "v_exp_f32 v215, v215\n" \
  "v_exp_f32 v216, v216\n" \
  "v_exp_f32 v217, v217\n" \
  "v_smfmac_f32_32x32x32_bf16 v[232:247], v[76:79], v[208:215], v252\n" \
  "ds_write_b128 v254, v[0:3] offset:16384\n" \
  "v_exp_f32 v218, v218\n" \
  "v_exp_f32 v219, v219\n" \
  "v_exp_f32 v220, v220\n" \
  "v_mfma_f32_32x32x16_bf16 v[200:215], v[16:19], v[248:251], 0\n" \
  "v_exp_f32 v221, v221\n" \
  "v_exp_f32 v222, v222\n" \
  "v_exp_f32 v223, v223\n" \
  "v_exp_f32 v224, v224\n" \
  "v_exp_f32 v225, v225\n" \
  "v_smfmac_f32_32x32x32_bf16 v[232:247], v[80:83], v[216:223], v252\n" \
  "ds_write_b128 v254, v[4:7] offset:17408\n" \
  "v_exp_f32 v226, v226\n" \
  "v_exp_f32 v227, v227\n" \
  "v_exp_f32 v228, v228\n" \
  "v_exp_f32 v229, v229\n" \
  "v_exp_f32 v230, v230\n" \
  "v_exp_f32 v231, v231\n" \
  "v_exp_f32 v200, v200\n" \
  "v_exp_f32 v201, v201\n" \
  "v_smfmac_f32_32x32x32_bf16 v[232:247], v[84:87], v[224:231], v252\n" \
  "ds_write_b32 v254, v255 offset:18432\n" \
  "v_exp_f32 v202, v202\n" \
  "v_exp_f32 v203, v203\n" \
  "v_exp_f32 v204, v204\n" \
  "v_mfma_f32_32x32x16_bf16 v[216:231], v[20:23], v[248:251], 0\n" \
  "v_exp_f32 v205, v205\n" \
  "v_exp_f32 v206, v206\n" \
  "v_exp_f32 v207, v207\n" \
  "v_exp_f32 v208, v208\n" \
  "v_exp_f32 v209, v209\n" \
  "v_smfmac_f32_32x32x32_bf16 v[232:247], v[88:91], v[200:207], v252\n" \
  "v_exp_f32 v210, v210\n" \
  "v_exp_f32 v211, v211\n" \
  "v_exp_f32 v212, v212\n" \
  "v_exp_f32 v213, v213\n" \
  "v_exp_f32 v214, v214\n" \
  "v_exp_f32 v215, v215\n" \
  "v_exp_f32 v216, v216\n" \
  "v_exp_f32 v217, v217\n" \
  "v_smfmac_f32_32x32x32_bf16 v[232:247], v[92:95], v[208:215], v252\n" \
  "v_exp_f32 v218, v218\n" \
  "v_exp_f32 v219, v219\n" \
  "v_exp_f32 v220, v220\n" \
  "v_mfma_f32_32x32x16_bf16 v[200:215], v[24:27], v[248:251], 0\n" \
  "v_exp_f32 v221, v221\n" \
  "v_exp_f32 v222, v222\n" \
  "v_exp_f32 v223, v223\n" \
  "v_exp_f32 v224, v224\n" \
  "v_exp_f32 v225, v225\n" \
  "v_smfmac_f32_32x32x32_bf16 v[232:247], v[96:99], v[216:223], v252\n" \
  "v_exp_f32 v226, v226\n" \
  "v_exp_f32 v227, v227\n" \
  "v_exp_f32 v228, v228\n" \
  "v_exp_f32 v229, v229\n" \
  "v_exp_f32 v230, v230\n" \
  "v_exp_f32 v231, v231\n" \
  "v_exp_f32 v200, v200\n" \
  "v_exp_f32 v201, v201\n" \
  "v_smfmac_f32_32x32x32_bf16 v[232:247], v[100:103], v[224:231], v252\n" \
  "v_exp_f32 v202, v202\n" \
  "v_exp_f32 v203, v203\n" \
  "v_exp_f32 v204, v204\n" \
  "v_mfma_f32_32x32x16_bf16 v[216:231], v[28:31], v[248:251], 0\n" \
  "v_exp_f32 v205, v205\n" \
  "v_exp_f32 v206, v206\n" \
  "v_exp_f32 v207, v207\n" \
  "v_exp_f32 v208, v208\n" \
  "v_exp_f32 v209, v209\n" \
  "v_smfmac_f32_32x32x32_bf16 v[232:247], v[104:107], v[200:207], v252\n" \
  "v_exp_f32 v210, v210\n" \
  "v_exp_f32 v211, v211\n" \
  "v_exp_f32 v212, v212\n" \
  "v_exp_f32 v213, v213\n" \
  "v_exp_f32 v214, v214\n" \
  "v_exp_f32 v215, v215\n" \
  "v_exp_f32 v216, v216\n" \
  "v_exp_f32 v217, v217\n" \
  "v_smfmac_f32_32x32x32_bf16 v[232:247], v[108:111], v[208:215], v252\n" \
  "v_exp_f32 v218, v218\n" \
  "v_exp_f32 v219, v219\n" \
  "v_exp_f32 v220, v220\n" \
  "v_mfma_f32_32x32x16_bf16 v[200:215], v[32:35], v[248:251], 0\n" \
  "v_exp_f32 v221, v221\n" \
  "v_exp_f32 v222, v222\n" \
  "v_exp_f32 v223, v223\n" \
  "v_exp_f32 v224, v224\n" \
  "v_exp_f32 v225, v225\n" \
  "v_smfmac_f32_32x32x32_bf16 v[232:247], v[112:115], v[216:223], v252\n" \
  "v_exp_f32 v226, v226\n" \
  "v_exp_f32 v227, v227\n" \
  "v_exp_f32 v228, v228\n" \
  "v_exp_f32 v229, v229\n" \
  "v_exp_f32 v230, v230\n" \
  "v_exp_f32 v231, v231\n" \
  "v_exp_f32 v200, v200\n" \
  "v_exp_f32 v201, v201\n" \
  "v_smfmac_f32_32x32x32_bf16 v[232:247], v[116:119], v[224:231], v252\n" \
  "v_exp_f32 v202, v202\n" \
  "v_exp_f32 v203, v203\n" \
  "v_exp_f32 v204, v204\n" \
  "v_mfma_f32_32x32x16_bf16 v[216:231], v[36:39], v[248:251], 0\n" \
  "v_exp_f32 v205, v205\n" \
  "v_exp_f32 v206, v206\n" \
  "v_exp_f32 v207, v207\n" \
  "v_exp_f32 v208, v208\n" \
  "v_exp_f32 v209, v209\n" \
  "v_smfmac_f32_32x32x32_bf16 v[232:247], v[120:123], v[200:207], v252\n" \
  "v_exp_f32 v210, v210\n" \
  "v_exp_f32 v211, v211\n" \
  "v_exp_f32 v212, v212\n" \
  "v_exp_f32 v213, v213\n" \
  "v_exp_f32 v214, v214\n" \
  "v_exp_f32 v215, v215\n" \
  "v_exp_f32 v216, v216\n" \
  "v_exp_f32 v217, v217\n" \
  "v_smfmac_f32_32x32x32_bf16 v[232:247], v[124:127], v[208:215], v252\n" \
  "v_exp_f32 v218, v218\n" \
  "v_exp_f32 v219, v219\n" \
  "v_exp_f32 v220, v220\n" \
  "v_mfma_f32_32x32x16_bf16 v[200:215], v[40:43], v[248:251], 0\n" \
  "v_exp_f32 v221, v221\n" \
  "v_exp_f32 v222, v222\n" \
  "v_exp_f32 v223, v223\n" \
  "v_exp_f32 v224, v224\n" \
  "v_exp_f32 v225, v225\n" \
  "v_smfmac_f32_32x32x32_bf16 v[232:247], v[128:131], v[216:223], v252\n" \
  "v_exp_f32 v226, v226\n" \
  "v_exp_f32 v227, v227\n" \
  "v_exp_f32 v228, v228\n" \
  "v_exp_f32 v229, v229\n" \
  "v_exp_f32 v230, v230\n" \
  "v_exp_f32 v231, v231\n" \
  "v_exp_f32 v200, v200\n" \
  "v_exp_f32 v201, v201\n" \
  "v_smfmac_f32_32x32x32_bf16 v[232:247], v[132:135], v[224:231], v252\n" \
  "v_exp_f32 v202, v202\n" \
  "v_exp_f32 v203, v203\n" \
  "v_exp_f32 v204, v204\n" \
  "v_mfma_f32_32x32x16_bf16 v[216:231], v[44:47], v[248:251], 0\n" \
  "v_exp_f32 v205, v205\n" \
  "v_exp_f32 v206, v206\n" \
  "v_exp_f32 v207, v207\n" \
  "v_exp_f32 v208, v208\n" \
  "v_exp_f32 v209, v209\n" \
  "v_smfmac_f32_32x32x32_bf16 v[232:247], v[136:139], v[200:207], v252\n" \
  "s_waitcnt lgkmcnt(0)\n" \
  "s_barrier\n" \
  "v_exp_f32 v210, v210\n" \
  "v_exp_f32 v211, v211\n" \
  "v_exp_f32 v212, v212\n" \
  "v_exp_f32 v213, v213\n" \
  "v_exp_f32 v214, v214\n" \
  "v_exp_f32 v215, v215\n" \
  "v_exp_f32 v216, v216\n" \
  "v_exp_f32 v217, v217\n" \
  "v_smfmac_f32_32x32x32_bf16 v[232:247], v[140:143], v[208:215], v252\n" \
  "v_exp_f32 v218, v218\n" \
  "v_exp_f32 v219, v219\n" \
  "v_exp_f32 v220, v220\n" \
  "v_mfma_f32_32x32x16_bf16 v[200:215], v[48:51], v[248:251], 0\n" \
  "v_exp_f32 v221, v221\n" \
  "v_exp_f32 v222, v222\n" \
  "v_exp_f32 v223, v223\n" \
  "v_exp_f32 v224, v224\n" \
  "v_exp_f32 v225, v225\n" \
  "v_smfmac_f32_32x32x32_bf16 v[232:247], v[144:147], v[216:223], v252\n" \
  "v_exp_f32 v226, v226\n" \
  "v_exp_f32 v227, v227\n" \
  "v_exp_f32 v228, v228\n" \
  "v_exp_f32 v229, v229\n" \
  "v_exp_f32 v230, v230\n" \
  "v_exp_f32 v231, v231\n" \
  "v_exp_f32 v200, v200\n" \
  "v_exp_f32 v201, v201\n" \
  "v_smfmac_f32_32x32x32_bf16 v[232:247], v[148:151], v[224:231], v252\n" \
  "v_exp_f32 v202, v202\n" \
  "v_exp_f32 v203, v203\n" \
  "v_exp_f32 v204, v204\n" \
  "v_mfma_f32_32x32x16_bf16 v[216:231], v[52:55], v[248:251], 0\n" \
  "v_exp_f32 v205, v205\n" \
  "v_exp_f32 v206, v206\n" \
  "v_exp_f32 v207, v207\n" \
  "v_exp_f32 v208, v208\n" \
  "v_exp_f32 v209, v209\n" \
  "v_smfmac_f32_32x32x32_bf16 v[232:247], v[152:155], v[200:207], v252\n" \
  "v_exp_f32 v210, v210\n" \
  "v_exp_f32 v211, v211\n" \
  "v_exp_f32 v212, v212\n" \
  "v_exp_f32 v213, v213\n" \
  "v_exp_f32 v214, v214\n" \
  "v_exp_f32 v215, v215\n" \
  "v_exp_f32 v216, v216\n" \
  "v_exp_f32 v217, v217\n" \
  "v_smfmac_f32_32x32x32_bf16 v[232:247], v[156:159], v[208:215], v252\n" \
  "v_exp_f32 v218, v218\n" \
  "v_exp_f32 v219, v219\n" \
  "v_exp_f32 v220, v220\n" \
  "v_mfma_f32_32x32x16_bf16 v[200:215], v[56:59], v[248:251], 0\n" \
  "v_exp_f32 v221, v221\n" \
  "v_exp_f32 v222, v222\n" \
  "v_exp_f32 v223, v223\n" \
  "v_exp_f32 v224, v224\n" \
  "v_exp_f32 v225, v225\n" \
  "v_smfmac_f32_32x32x32_bf16 v[232:247], v[160:163], v[216:223], v252\n" \
  "v_exp_f32 v226, v226\n" \
  "v_exp_f32 v227, v227\n" \
  "v_exp_f32 v228, v228\n" \
  "v_exp_f32 v229, v229\n" \
  "v_exp_f32 v230, v230\n" \
  "v_exp_f32 v231, v231\n" \
  "v_exp_f32 v200, v200\n" \
  "v_exp_f32 v201, v201\n" \
  "v_smfmac_f32_32x32x32_bf16 v[232:247], v[164:167], v[224:231], v252\n" \
  "v_exp_f32 v202, v202\n" \
  "v_exp_f32 v203, v203\n" \
  "v_exp_f32 v204, v204\n" \
  "v_mfma_f32_32x32x16_bf16 v[216:231], v[60:63], v[248:251], 0\n" \
  "v_exp_f32 v205, v205\n" \
  "v_exp_f32 v206, v206\n" \
  "v_exp_f32 v207, v207\n" \
  "v_exp_f32 v208, v208\n" \
  "v_exp_f32 v209, v209\n" \
  "v_smfmac_f32_32x32x32_bf16 v[232:247], v[168:171], v[200:207], v252\n" \
  "v_exp_f32 v210, v210\n" \
  "v_exp_f32 v211, v211\n" \
  "v_exp_f32 v212, v212\n" \
  "v_exp_f32 v213, v213\n" \
  "v_exp_f32 v214, v214\n" \
  "v_exp_f32 v215, v215\n" \
  "v_exp_f32 v216, v216\n" \
  "v_exp_f32 v217, v217\n" \
  "v_smfmac_f32_32x32x32_bf16 v[232:247], v[172:175], v[208:215], v252\n" \
  "v_exp_f32 v218, v218\n" \
  "v_exp_f32 v219, v219\n" \
  "v_exp_f32 v220, v220\n" \
  "v_mfma_f32_32x32x16_bf16 v[200:215], v[64:67], v[248:251], 0\n" \
  "v_exp_f32 v221, v221\n" \
  "v_exp_f32 v222, v222\n" \
  "v_exp_f32 v223, v223\n" \
  "v_exp_f32 v224, v224\n" \
  "v_exp_f32 v225, v225\n" \
  "v_smfmac_f32_32x32x32_bf16 v[232:247], v[176:179], v[216:223], v252\n" \
  "v_exp_f32 v226, v226\n" \
  "v_exp_f32 v227, v227\n" \
  "v_exp_f32 v228, v228\n" \
  "v_exp_f32 v229, v229\n" \
  "v_exp_f32 v230, v230\n" \
  "v_exp_f32 v231, v231\n" \
  "v_exp_f32 v200, v200\n" \
  "v_exp_f32 v201, v201\n" \
  "v_smfmac_f32_32x32x32_bf16 v[232:247], v[180:183], v[224:231], v252\n" \
  "v_exp_f32 v202, v202\n" \
  "v_exp_f32 v203, v203\n" \
  "v_exp_f32 v204, v204\n" \
  "v_mfma_f32_32x32x16_bf16 v[216:231], v[68:71], v[248:251], 0\n" \
  "ds_read_b128 v[248:251], v253 offset:12288\n" \
  "v_exp_f32 v205, v205\n" \
  "v_exp_f32 v206, v206\n" \
  "v_exp_f32 v207, v207\n" \
  "v_exp_f32 v208, v208\n" \
  "v_exp_f32 v209, v209\n" \
  "v_smfmac_f32_32x32x32_bf16 v[232:247], v[184:187], v[200:207], v252\n" \
  "v_exp_f32 v210, v210\n" \
  "v_exp_f32 v211, v211\n" \
  "v_exp_f32 v212, v212\n" \
  "v_exp_f32 v213, v213\n" \
  "v_exp_f32 v214, v214\n" \
  "v_exp_f32 v215, v215\n" \
  "v_exp_f32 v216, v216\n" \
  "v_exp_f32 v217, v217\n" \
  "v_smfmac_f32_32x32x32_bf16 v[232:247], v[188:191], v[208:215], v252\n" \
  "s_waitcnt lgkmcnt(0)\n" \
  "v_exp_f32 v218, v218\n" \
  "v_exp_f32 v219, v219\n" \
  "v_exp_f32 v220, v220\n" \
  "v_mfma_f32_32x32x16_bf16 v[200:215], v[8:11], v[248:251], 0\n" \
  "v_exp_f32 v221, v221\n" \
  "v_exp_f32 v222, v222\n" \
  "v_exp_f32 v223, v223\n" \
  "v_exp_f32 v224, v224\n" \
  "v_exp_f32 v225, v225\n" \
  "v_smfmac_f32_32x32x32_bf16 v[232:247], v[192:195], v[216:223], v252\n" \
  "v_exp_f32 v226, v226\n" \
  "v_exp_f32 v227, v227\n" \
  "v_exp_f32 v228, v228\n" \
  "v_exp_f32 v229, v229\n" \
  "v_exp_f32 v230, v230\n" \
  "v_exp_f32 v231, v231\n" \
  "v_exp_f32 v200, v200\n" \
  "v_exp_f32 v201, v201\n" \
  "v_smfmac_f32_32x32x32_bf16 v[232:247], v[196:199], v[224:231], v252\n" \
  "v_exp_f32 v202, v202\n" \
  "v_exp_f32 v203, v203\n" \
  "v_exp_f32 v204, v204\n" \
  "v_mfma_f32_32x32x16_bf16 v[216:231], v[12:15], v[248:251], 0\n" \
  "v_exp_f32 v205, v205\n" \
  "v_exp_f32 v206, v206\n" \
  "v_exp_f32 v207, v207\n" \
  "v_exp_f32 v208, v208\n" \
  "v_exp_f32 v209, v209\n" \
  "v_exp_f32 v210, v210\n" \
  "v_exp_f32 v211, v211\n" \
  "v_exp_f32 v212, v212\n" \
  "v_mov_b64 v[0:1], v[232:233]\n" \
  "v_mov_b64 v[2:3], v[234:235]\n" \
  "v_mov_b64 v[4:5], v[236:237]\n" \
  "v_mov_b64 v[6:7], v[238:239]\n" \
  "v_mov_b32 v255, v240\n" \
  "v_mov_b64 v[232:233], 0\n" \
  "v_mov_b64 v[234:235], 0\n" \
  "v_mov_b64 v[236:237], 0\n" \
  "v_mov_b64 v[238:239], 0\n" \
  "v_mov_b64 v[240:241], 0\n" \
  "v_exp_f32 v213, v213\n" \
  "v_exp_f32 v214, v214\n" \
  "v_smfmac_f32_32x32x32_bf16 v[232:247], v[72:75], v[200:207], v252\n" \
  "v_exp_f32 v215, v215\n" \
  "v_exp_f32 v216, v216\n" \
  "v_exp_f32 v217, v217\n" \
  "v_smfmac_f32_32x32x32_bf16 v[232:247], v[76:79], v[208:215], v252\n" \
  "ds_write_b128 v254, v[0:3] offset:28672\n" \
  "v_exp_f32 v218, v218\n" \
  "v_exp_f32 v219, v219\n" \
  "v_exp_f32 v220, v220\n" \
  "v_mfma_f32_32x32x16_bf16 v[200:215], v[16:19], v[248:251], 0\n" \
  "v_exp_f32 v221, v221\n" \
  "v_exp_f32 v222, v222\n" \
  "v_exp_f32 v223, v223\n" \
  "v_exp_f32 v224, v224\n" \
  "v_exp_f32 v225, v225\n" \
  "v_smfmac_f32_32x32x32_bf16 v[232:247], v[80:83], v[216:223], v252\n" \
  "ds_write_b128 v254, v[4:7] offset:29696\n" \
  "v_exp_f32 v226, v226\n" \
  "v_exp_f32 v227, v227\n" \
  "v_exp_f32 v228, v228\n" \
  "v_exp_f32 v229, v229\n" \
  "v_exp_f32 v230, v230\n" \
  "v_exp_f32 v231, v231\n" \
  "v_exp_f32 v200, v200\n" \
  "v_exp_f32 v201, v201\n" \
  "v_smfmac_f32_32x32x32_bf16 v[232:247], v[84:87], v[224:231], v252\n" \
  "ds_write_b32 v254, v255 offset:30720\n" \
  "v_exp_f32 v202, v202\n" \
  "v_exp_f32 v203, v203\n" \
  "v_exp_f32 v204, v204\n" \
  "v_mfma_f32_32x32x16_bf16 v[216:231], v[20:23], v[248:251], 0\n" \
  "v_exp_f32 v205, v205\n" \
  "v_exp_f32 v206, v206\n" \
  "v_exp_f32 v207, v207\n" \
  "v_exp_f32 v208, v208\n" \
  "v_exp_f32 v209, v209\n" \
  "v_smfmac_f32_32x32x32_bf16 v[232:247], v[88:91], v[200:207], v252\n" \
  "v_exp_f32 v210, v210\n" \
  "v_exp_f32 v211, v211\n" \
  "v_exp_f32 v212, v212\n" \
  "v_exp_f32 v213, v213\n" \
  "v_exp_f32 v214, v214\n" \
  "v_exp_f32 v215, v215\n" \
  "v_exp_f32 v216, v216\n" \
  "v_exp_f32 v217, v217\n" \
  "v_smfmac_f32_32x32x32_bf16 v[232:247], v[92:95], v[208:215], v252\n" \
  "v_exp_f32 v218, v218\n" \
  "v_exp_f32 v219, v219\n" \
  "v_exp_f32 v220, v220\n" \
  "v_mfma_f32_32x32x16_bf16 v[200:215], v[24:27], v[248:251], 0\n" \
  "v_exp_f32 v221, v221\n" \
  "v_exp_f32 v222, v222\n" \
  "v_exp_f32 v223, v223\n" \
  "v_exp_f32 v224, v224\n" \
  "v_exp_f32 v225, v225\n" \
  "v_smfmac_f32_32x32x32_bf16 v[232:247], v[96:99], v[216:223], v252\n" \
  "v_exp_f32 v226, v226\n" \
  "v_exp_f32 v227, v227\n" \
  "v_exp_f32 v228, v228\n" \
  "v_exp_f32 v229, v229\n" \
  "v_exp_f32 v230, v230\n" \
  "v_exp_f32 v231, v231\n" \
  "v_exp_f32 v200, v200\n" \
  "v_exp_f32 v201, v201\n" \
  "v_smfmac_f32_32x32x32_bf16 v[232:247], v[100:103], v[224:231], v252\n" \
  "v_exp_f32 v202, v202\n" \
  "v_exp_f32 v203, v203\n" \
  "v_exp_f32 v204, v204\n" \
  "v_mfma_f32_32x32x16_bf16 v[216:231], v[28:31], v[248:251], 0\n" \
  "v_exp_f32 v205, v205\n" \
  "v_exp_f32 v206, v206\n" \
  "v_exp_f32 v207, v207\n" \
  "v_exp_f32 v208, v208\n" \
  "v_exp_f32 v209, v209\n" \
  "v_smfmac_f32_32x32x32_bf16 v[232:247], v[104:107], v[200:207], v252\n" \
  "v_exp_f32 v210, v210\n" \
  "v_exp_f32 v211, v211\n" \
  "v_exp_f32 v212, v212\n" \
  "v_exp_f32 v213, v213\n" \
  "v_exp_f32 v214, v214\n" \
  "v_exp_f32 v215, v215\n" \
  "v_exp_f32 v216, v216\n" \
  "v_exp_f32 v217, v217\n" \
  "v_smfmac_f32_32x32x32_bf16 v[232:247], v[108:111], v[208:215], v252\n" \
  "v_exp_f32 v218, v218\n" \
  "v_exp_f32 v219, v219\n" \
  "v_exp_f32 v220, v220\n" \
  "v_mfma_f32_32x32x16_bf16 v[200:215], v[32:35], v[248:251], 0\n" \
  "v_exp_f32 v221, v221\n" \
  "v_exp_f32 v222, v222\n" \
  "v_exp_f32 v223, v223\n" \
  "v_exp_f32 v224, v224\n" \
  "v_exp_f32 v225, v225\n" \
  "v_smfmac_f32_32x32x32_bf16 v[232:247], v[112:115], v[216:223], v252\n" \
  "v_exp_f32 v226, v226\n" \
  "v_exp_f32 v227, v227\n" \
  "v_exp_f32 v228, v228\n" \
  "v_exp_f32 v229, v229\n" \
  "v_exp_f32 v230, v230\n" \
  "v_exp_f32 v231, v231\n" \
  "v_exp_f32 v200, v200\n" \
  "v_exp_f32 v201, v201\n" \
  "v_smfmac_f32_32x32x32_bf16 v[232:247], v[116:119], v[224:231], v252\n" \
  "v_exp_f32 v202, v202\n" \
  "v_exp_f32 v203, v203\n" \
  "v_exp_f32 v204, v204\n" \
  "v_mfma_f32_32x32x16_bf16 v[216:231], v[36:39], v[248:251], 0\n" \
  "v_exp_f32 v205, v205\n" \
  "v_exp_f32 v206, v206\n" \
  "v_exp_f32 v207, v207\n" \
  "v_exp_f32 v208, v208\n" \
  "v_exp_f32 v209, v209\n" \
  "v_smfmac_f32_32x32x32_bf16 v[232:247], v[120:123], v[200:207], v252\n" \
  "v_exp_f32 v210, v210\n" \
  "v_exp_f32 v211, v211\n" \
  "v_exp_f32 v212, v212\n" \
  "v_exp_f32 v213, v213\n" \
  "v_exp_f32 v214, v214\n" \
  "v_exp_f32 v215, v215\n" \
  "v_exp_f32 v216, v216\n" \
  "v_exp_f32 v217, v217\n" \
  "v_smfmac_f32_32x32x32_bf16 v[232:247], v[124:127], v[208:215], v252\n" \
  "v_exp_f32 v218, v218\n" \
  "v_exp_f32 v219, v219\n" \
  "v_exp_f32 v220, v220\n" \
  "v_mfma_f32_32x32x16_bf16 v[200:215], v[40:43], v[248:251], 0\n" \
  "v_exp_f32 v221, v221\n" \
  "v_exp_f32 v222, v222\n" \
  "v_exp_f32 v223, v223\n" \
  "v_exp_f32 v224, v224\n" \
  "v_exp_f32 v225, v225\n" \
  "v_smfmac_f32_32x32x32_bf16 v[232:247], v[128:131], v[216:223], v252\n" \
  "s_waitcnt lgkmcnt(0)\n" \
  "s_barrier\n" \
  "v_exp_f32 v226, v226\n" \
  "v_exp_f32 v227, v227\n" \
  "v_exp_f32 v228, v228\n" \
  "v_exp_f32 v229, v229\n" \
  "v_exp_f32 v230, v230\n" \
  "v_exp_f32 v231, v231\n" \
  "v_exp_f32 v200, v200\n" \
  "v_exp_f32 v201, v201\n" \
  "v_smfmac_f32_32x32x32_bf16 v[232:247], v[132:135], v[224:231], v252\n" \
  "v_exp_f32 v202, v202\n" \
  "v_exp_f32 v203, v203\n" \
  "v_exp_f32 v204, v204\n" \
  "v_mfma_f32_32x32x16_bf16 v[216:231], v[44:47], v[248:251], 0\n" \
  "v_exp_f32 v205, v205\n" \
  "v_exp_f32 v206, v206\n" \
  "v_exp_f32 v207, v207\n" \
  "v_exp_f32 v208, v208\n" \
  "v_exp_f32 v209, v209\n" \
  "v_smfmac_f32_32x32x32_bf16 v[232:247], v[136:139], v[200:207], v252\n" \
  "v_exp_f32 v210, v210\n" \
  "v_exp_f32 v211, v211\n" \
  "v_exp_f32 v212, v212\n" \
  "v_exp_f32 v213, v213\n" \
  "v_exp_f32 v214, v214\n" \
  "v_exp_f32 v215, v215\n" \
  "v_exp_f32 v216, v216\n" \
  "v_exp_f32 v217, v217\n" \
  "v_smfmac_f32_32x32x32_bf16 v[232:247], v[140:143], v[208:215], v252\n" \
  "v_exp_f32 v218, v218\n" \
  "v_exp_f32 v219, v219\n" \
  "v_exp_f32 v220, v220\n" \
  "v_mfma_f32_32x32x16_bf16 v[200:215], v[48:51], v[248:251], 0\n" \
  "v_exp_f32 v221, v221\n" \
  "v_exp_f32 v222, v222\n" \
  "v_exp_f32 v223, v223\n" \
  "v_exp_f32 v224, v224\n" \
  "v_exp_f32 v225, v225\n" \
  "v_smfmac_f32_32x32x32_bf16 v[232:247], v[144:147], v[216:223], v252\n" \
  "v_exp_f32 v226, v226\n" \
  "v_exp_f32 v227, v227\n" \
  "v_exp_f32 v228, v228\n" \
  "v_exp_f32 v229, v229\n" \
  "v_exp_f32 v230, v230\n" \
  "v_exp_f32 v231, v231\n" \
  "v_exp_f32 v200, v200\n" \
  "v_exp_f32 v201, v201\n" \
  "v_smfmac_f32_32x32x32_bf16 v[232:247], v[148:151], v[224:231], v252\n" \
  "v_exp_f32 v202, v202\n" \
  "v_exp_f32 v203, v203\n" \
  "v_exp_f32 v204, v204\n" \
  "v_mfma_f32_32x32x16_bf16 v[216:231], v[52:55], v[248:251], 0\n" \
  "v_exp_f32 v205, v205\n" \
  "v_exp_f32 v206, v206\n" \
  "v_exp_f32 v207, v207\n" \
  "v_exp_f32 v208, v208\n" \
  "v_exp_f32 v209, v209\n" \
  "v_smfmac_f32_32x32x32_bf16 v[232:247], v[152:155], v[200:207], v252\n" \
  "v_exp_f32 v210, v210\n" \
  "v_exp_f32 v211, v211\n" \
  "v_exp_f32 v212, v212\n" \
  "v_exp_f32 v213, v213\n" \
  "v_exp_f32 v214, v214\n" \
  "v_exp_f32 v215, v215\n" \
  "v_exp_f32 v216, v216\n" \
  "v_exp_f32 v217, v217\n" \
  "v_smfmac_f32_32x32x32_bf16 v[232:247], v[156:159], v[208:215], v252\n" \
  "v_exp_f32 v218, v218\n" \
  "v_exp_f32 v219, v219\n" \
  "v_exp_f32 v220, v220\n" \
  "v_mfma_f32_32x32x16_bf16 v[200:215], v[56:59], v[248:251], 0\n" \
  "v_exp_f32 v221, v221\n" \
  "v_exp_f32 v222, v222\n" \
  "v_exp_f32 v223, v223\n" \
  "v_exp_f32 v224, v224\n" \
  "v_exp_f32 v225, v225\n" \
  "v_smfmac_f32_32x32x32_bf16 v[232:247], v[160:163], v[216:223], v252\n" \
  "v_exp_f32 v226, v226\n" \
  "v_exp_f32 v227, v227\n" \
  "v_exp_f32 v228, v228\n" \
  "v_exp_f32 v229, v229\n" \
  "v_exp_f32 v230, v230\n" \
  "v_exp_f32 v231, v231\n" \
  "v_exp_f32 v200, v200\n" \
  "v_exp_f32 v201, v201\n" \
  "v_smfmac_f32_32x32x32_bf16 v[232:247], v[164:167], v[224:231], v252\n" \
  "v_exp_f32 v202, v202\n" \
  "v_exp_f32 v203, v203\n" \
  "v_exp_f32 v204, v204\n" \
  "v_mfma_f32_32x32x16_bf16 v[216:231], v[60:63], v[248:251], 0\n" \
  "v_exp_f32 v205, v205\n" \
  "v_exp_f32 v206, v206\n" \
  "v_exp_f32 v207, v207\n" \
  "v_exp_f32 v208, v208\n" \
  "v_exp_f32 v209, v209\n" \
  "v_smfmac_f32_32x32x32_bf16 v[232:247], v[168:171], v[200:207], v252\n" \
  "v_exp_f32 v210, v210\n" \
  "v_exp_f32 v211, v211\n" \
  "v_exp_f32 v212, v212\n" \
  "v_exp_f32 v213, v213\n" \
  "v_exp_f32 v214, v214\n" \
  "v_exp_f32 v215, v215\n" \
  "v_exp_f32 v216, v216\n" \
  "v_exp_f32 v217, v217\n" \
  "v_smfmac_f32_32x32x32_bf16 v[232:247], v[172:175], v[208:215], v252\n" \
  "v_exp_f32 v218, v218\n" \
  "v_exp_f32 v219, v219\n" \
  "v_exp_f32 v220, v220\n" \
  "v_mfma_f32_32x32x16_bf16 v[200:215], v[64:67], v[248:251], 0\n" \
  "v_exp_f32 v221, v221\n" \
  "v_exp_f32 v222, v222\n" \
  "v_exp_f32 v223, v223\n" \
  "v_exp_f32 v224, v224\n" \
  "v_exp_f32 v225, v225\n" \
  "v_smfmac_f32_32x32x32_bf16 v[232:247], v[176:179], v[216:223], v252\n" \
  "v_exp_f32 v226, v226\n" \
  "v_exp_f32 v227, v227\n" \
  "v_exp_f32 v228, v228\n" \
  "v_exp_f32 v229, v229\n" \
  "v_exp_f32 v230, v230\n" \
  "v_exp_f32 v231, v231\n" \
  "v_exp_f32 v200, v200\n" \
  "v_exp_f32 v201, v201\n" \
  "v_smfmac_f32_32x32x32_bf16 v[232:247], v[180:183], v[224:231], v252\n" \
  ".Lpad2_%=:\n" \
  "s_cbranch_execz .Lpad3_%=\n" \
  "v_exp_f32 v202, v202\n" \
  "v_exp_f32 v203, v203\n" \
  "v_exp_f32 v204, v204\n" \
  "v_mfma_f32_32x32x16_bf16 v[216:231], v[68:71], v[248:251], 0\n" \
  "ds_read_b128 v[248:251], v253 offset:13312\n" \
  "v_exp_f32 v205, v205\n" \
  "v_exp_f32 v206, v206\n" \
  "v_exp_f32 v207, v207\n" \
  "v_exp_f32 v208, v208\n" \
  "v_exp_f32 v209, v209\n" \
  "v_smfmac_f32_32x32x32_bf16 v[232:247], v[184:187], v[200:207], v252\n" \
  "v_exp_f32 v210, v210\n" \
  "v_exp_f32 v211, v211\n" \
  "v_exp_f32 v212, v212\n" \
  "v_exp_f32 v213, v213\n" \
  "v_exp_f32 v214, v214\n" \
  "v_exp_f32 v215, v215\n" \
  "v_exp_f32 v216, v216\n" \
  "v_exp_f32 v217, v217\n" \
  "v_smfmac_f32_32x32x32_bf16 v[232:247], v[188:191], v[208:215], v252\n" \
  "s_waitcnt lgkmcnt(0)\n" \
  "v_exp_f32 v218, v218\n" \
  "v_exp_f32 v219, v219\n" \
  "v_exp_f32 v220, v220\n" \
  "v_mfma_f32_32x32x16_bf16 v[200:215], v[8:11], v[248:251], 0\n" \
  "v_exp_f32 v221, v221\n" \
  "v_exp_f32 v222, v222\n" \
  "v_exp_f32 v223, v223\n" \
  "v_exp_f32 v224, v224\n" \
  "v_exp_f32 v225, v225\n" \
  "v_smfmac_f32_32x32x32_bf16 v[232:247], v[192:195], v[216:223], v252\n" \
  "v_exp_f32 v226, v226\n" \
  "v_exp_f32 v227, v227\n" \
  "v_exp_f32 v228, v228\n" \
  "v_exp_f32 v229, v229\n" \
  "v_exp_f32 v230, v230\n" \
  "v_exp_f32 v231, v231\n" \
  "v_exp_f32 v200, v200\n" \
  "v_exp_f32 v201, v201\n" \
  "v_smfmac_f32_32x32x32_bf16 v[232:247], v[196:199], v[224:231], v252\n" \
  "v_exp_f32 v202, v202\n" \
  "v_exp_f32 v203, v203\n" \
  "v_exp_f32 v204, v204\n" \
  "v_mfma_f32_32x32x16_bf16 v[216:231], v[12:15], v[248:251], 0\n" \
  "v_exp_f32 v205, v205\n" \
  "v_exp_f32 v206, v206\n" \
  "v_exp_f32 v207, v207\n" \
  "v_exp_f32 v208, v208\n" \
  "v_exp_f32 v209, v209\n" \
  "v_exp_f32 v210, v210\n" \
  "v_exp_f32 v211, v211\n" \
  "v_exp_f32 v212, v212\n" \
  "v_mov_b64 v[0:1], v[232:233]\n" \
  "v_mov_b64 v[2:3], v[234:235]\n" \
  "v_mov_b64 v[4:5], v[236:237]\n" \
  "v_mov_b64 v[6:7], v[238:239]\n" \
  "v_mov_b32 v255, v240\n" \
  "v_mov_b64 v[232:233], 0\n" \
  "v_mov_b64 v[234:235], 0\n" \
  "v_mov_b64 v[236:237], 0\n" \
  "v_mov_b64 v[238:239], 0\n" \
  "v_mov_b64 v[240:241], 0\n" \
  "v_exp_f32 v213, v213\n" \
  "v_exp_f32 v214, v214\n" \
  "v_smfmac_f32_32x32x32_bf16 v[232:247], v[72:75], v[200:207], v252\n" \
  "v_exp_f32 v215, v215\n" \
  "v_exp_f32 v216, v216\n" \
  "v_exp_f32 v217, v217\n" \
  "v_smfmac_f32_32x32x32_bf16 v[232:247], v[76:79], v[208:215], v252\n" \
  "ds_write_b128 v254, v[0:3] offset:16384\n" \
  "v_exp_f32 v218, v218\n" \
  "v_exp_f32 v219, v219\n" \
  "v_exp_f32 v220, v220\n" \
  "v_mfma_f32_32x32x16_bf16 v[200:215], v[16:19], v[248:251], 0\n" \
  "v_exp_f32 v221, v221\n" \
  "v_exp_f32 v222, v222\n" \
  "v_exp_f32 v223, v223\n" \
  "v_exp_f32 v224, v224\n" \
  "v_exp_f32 v225, v225\n" \
  "v_smfmac_f32_32x32x32_bf16 v[232:247], v[80:83], v[216:223], v252\n" \
  "ds_write_b128 v254, v[4:7] offset:17408\n" \
  "v_exp_f32 v226, v226\n" \
  "v_exp_f32 v227, v227\n" \
  "v_exp_f32 v228, v228\n" \
  "v_exp_f32 v229, v229\n" \
  "v_exp_f32 v230, v230\n" \
  "v_exp_f32 v231, v231\n" \
  "v_exp_f32 v200, v200\n" \
  "v_exp_f32 v201, v201\n" \
  "v_smfmac_f32_32x32x32_bf16 v[232:247], v[84:87], v[224:231], v252\n" \
  "ds_write_b32 v254, v255 offset:18432\n" \
  "v_exp_f32 v202, v202\n" \
  "v_exp_f32 v203, v203\n" \
  "v_exp_f32 v204, v204\n" \
  "v_mfma_f32_32x32x16_bf16 v[216:231], v[20:23], v[248:251], 0\n" \
  "v_exp_f32 v205, v205\n" \
  "v_exp_f32 v206, v206\n" \
  "v_exp_f32 v207, v207\n" \
  "v_exp_f32 v208, v208\n" \
  "v_exp_f32 v209, v209\n" \
  "v_smfmac_f32_32x32x32_bf16 v[232:247], v[88:91], v[200:207], v252\n" \
  "v_exp_f32 v210, v210\n" \
  "v_exp_f32 v211, v211\n" \
  "v_exp_f32 v212, v212\n" \
  "v_exp_f32 v213, v213\n" \
  "v_exp_f32 v214, v214\n" \
  "v_exp_f32 v215, v215\n" \
  "v_exp_f32 v216, v216\n" \
  "v_exp_f32 v217, v217\n" \
  "v_smfmac_f32_32x32x32_bf16 v[232:247], v[92:95], v[208:215], v252\n" \
  "v_exp_f32 v218, v218\n" \
  "v_exp_f32 v219, v219\n" \
  "v_exp_f32 v220, v220\n" \
  "v_mfma_f32_32x32x16_bf16 v[200:215], v[24:27], v[248:251], 0\n" \
  "v_exp_f32 v221, v221\n" \
  "v_exp_f32 v222, v222\n" \
  "v_exp_f32 v223, v223\n" \
  "v_exp_f32 v224, v224\n" \
  "v_exp_f32 v225, v225\n" \
  "v_smfmac_f32_32x32x32_bf16 v[232:247], v[96:99], v[216:223], v252\n" \
  "v_exp_f32 v226, v226\n" \
  "v_exp_f32 v227, v227\n" \
  "v_exp_f32 v228, v228\n" \
  "v_exp_f32 v229, v229\n" \
  "v_exp_f32 v230, v230\n" \
  "v_exp_f32 v231, v231\n" \
  "v_exp_f32 v200, v200\n" \
  "v_exp_f32 v201, v201\n" \
  "v_smfmac_f32_32x32x32_bf16 v[232:247], v[100:103], v[224:231], v252\n" \
  "v_exp_f32 v202, v202\n" \
  "v_exp_f32 v203, v203\n" \
  "v_exp_f32 v204, v204\n" \
  "v_mfma_f32_32x32x16_bf16 v[216:231], v[28:31], v[248:251], 0\n" \
  "v_exp_f32 v205, v205\n" \
  "v_exp_f32 v206, v206\n" \
  "v_exp_f32 v207, v207\n" \
  "v_exp_f32 v208, v208\n" \
  "v_exp_f32 v209, v209\n" \
  "v_smfmac_f32_32x32x32_bf16 v[232:247], v[104:107], v[200:207], v252\n" \
  "v_exp_f32 v210, v210\n" \
  "v_exp_f32 v211, v211\n" \
  "v_exp_f32 v212, v212\n" \
  "v_exp_f32 v213, v213\n" \
  "v_exp_f32 v214, v214\n" \
  "v_exp_f32 v215, v215\n" \
  "v_exp_f32 v216, v216\n" \
  "v_exp_f32 v217, v217\n" \
  "v_smfmac_f32_32x32x32_bf16 v[232:247], v[108:111], v[208:215], v252\n" \
  "v_exp_f32 v218, v218\n" \
  "v_exp_f32 v219, v219\n" \
  "v_exp_f32 v220, v220\n" \
  "v_mfma_f32_32x32x16_bf16 v[200:215], v[32:35], v[248:251], 0\n" \
  "v_exp_f32 v221, v221\n" \
  "v_exp_f32 v222, v222\n" \
  "v_exp_f32 v223, v223\n" \
  "v_exp_f32 v224, v224\n" \
  "v_exp_f32 v225, v225\n" \
  "v_smfmac_f32_32x32x32_bf16 v[232:247], v[112:115], v[216:223], v252\n" \
  "v_exp_f32 v226, v226\n" \
  "v_exp_f32 v227, v227\n" \
  "v_exp_f32 v228, v228\n" \
  "v_exp_f32 v229, v229\n" \
  "v_exp_f32 v230, v230\n" \
  "v_exp_f32 v231, v231\n" \
  "v_exp_f32 v200, v200\n" \
  "v_exp_f32 v201, v201\n" \
  "v_smfmac_f32_32x32x32_bf16 v[232:247], v[116:119], v[224:231], v252\n" \
  "v_exp_f32 v202, v202\n" \
  "v_exp_f32 v203, v203\n" \
  "v_exp_f32 v204, v204\n" \
  "v_mfma_f32_32x32x16_bf16 v[216:231], v[36:39], v[248:251], 0\n" \
  "v_exp_f32 v205, v205\n" \
  "v_exp_f32 v206, v206\n" \
  "v_exp_f32 v207, v207\n" \
  "v_exp_f32 v208, v208\n" \
  "v_exp_f32 v209, v209\n" \
  "v_smfmac_f32_32x32x32_bf16 v[232:247], v[120:123], v[200:207], v252\n" \
  "s_waitcnt lgkmcnt(0)\n" \
  "s_barrier\n" \
  "v_exp_f32 v210, v210\n" \
  "v_exp_f32 v211, v211\n" \
  "v_exp_f32 v212, v212\n" \
  "v_exp_f32 v213, v213\n" \
  "v_exp_f32 v214, v214\n" \
  "v_exp_f32 v215, v215\n" \
  "v_exp_f32 v216, v216\n" \
  "v_exp_f32 v217, v217\n" \
  "v_smfmac_f32_32x32x32_bf16 v[232:247], v[124:127], v[208:215], v252\n" \
  "v_exp_f32 v218, v218\n" \
  "v_exp_f32 v219, v219\n" \
  "v_exp_f32 v220, v220\n" \
  "v_mfma_f32_32x32x16_bf16 v[200:215], v[40:43], v[248:251], 0\n" \
  "v_exp_f32 v221, v221\n" \
  "v_exp_f32 v222, v222\n" \
  "v_exp_f32 v223, v223\n" \
  "v_exp_f32 v224, v224\n" \
  "v_exp_f32 v225, v225\n" \
  "v_smfmac_f32_32x32x32_bf16 v[232:247], v[128:131], v[216:223], v252\n" \
  "v_exp_f32 v226, v226\n" \
  "v_exp_f32 v227, v227\n" \
  "v_exp_f32 v228, v228\n" \
  "v_exp_f32 v229, v229\n" \
  "v_exp_f32 v230, v230\n" \
  "v_exp_f32 v231, v231\n" \
  "v_exp_f32 v200, v200\n" \
  "v_exp_f32 v201, v201\n" \
  "v_smfmac_f32_32x32x32_bf16 v[232:247], v[132:135], v[224:231], v252\n" \
  "v_exp_f32 v202, v202\n" \
  "v_exp_f32 v203, v203\n" \
  "v_exp_f32 v204, v204\n" \
  "v_mfma_f32_32x32x16_bf16 v[216:231], v[44:47], v[248:251], 0\n" \
  "v_exp_f32 v205, v205\n" \
  "v_exp_f32 v206, v206\n" \
  "v_exp_f32 v207, v207\n" \
  "v_exp_f32 v208, v208\n" \
  "v_exp_f32 v209, v209\n" \
  "v_smfmac_f32_32x32x32_bf16 v[232:247], v[136:139], v[200:207], v252\n" \
  "v_exp_f32 v210, v210\n" \
  "v_exp_f32 v211, v211\n" \
  "v_exp_f32 v212, v212\n" \
  "v_exp_f32 v213, v213\n" \
  "v_exp_f32 v214, v214\n" \
  "v_exp_f32 v215, v215\n" \
  "v_exp_f32 v216, v216\n" \
  "v_exp_f32 v217, v217\n" \
  "v_smfmac_f32_32x32x32_bf16 v[232:247], v[140:143], v[208:215], v252\n" \
  "v_exp_f32 v218, v218\n" \
  "v_exp_f32 v219, v219\n" \
  "v_exp_f32 v220, v220\n" \
  "v_mfma_f32_32x32x16_bf16 v[200:215], v[48:51], v[248:251], 0\n" \
  "v_exp_f32 v221, v221\n" \
  "v_exp_f32 v222, v222\n" \
  "v_exp_f32 v223, v223\n" \
  "v_exp_f32 v224, v224\n" \
  "v_exp_f32 v225, v225\n" \
  "v_smfmac_f32_32x32x32_bf16 v[232:247], v[144:147], v[216:223], v252\n" \
  "v_exp_f32 v226, v226\n" \
  "v_exp_f32 v227, v227\n" \
  "v_exp_f32 v228, v228\n" \
  "v_exp_f32 v229, v229\n" \
  "v_exp_f32 v230, v230\n" \
  "v_exp_f32 v231, v231\n" \
  "v_exp_f32 v200, v200\n" \
  "v_exp_f32 v201, v201\n" \
  "v_smfmac_f32_32x32x32_bf16 v[232:247], v[148:151], v[224:231], v252\n" \
  "v_exp_f32 v202, v202\n" \
  "v_exp_f32 v203, v203\n" \
  "v_exp_f32 v204, v204\n" \
  "v_mfma_f32_32x32x16_bf16 v[216:231], v[52:55], v[248:251], 0\n" \
  "v_exp_f32 v205, v205\n" \
  "v_exp_f32 v206, v206\n" \
  "v_exp_f32 v207, v207\n" \
  "v_exp_f32 v208, v208\n" \
  "v_exp_f32 v209, v209\n" \
  "v_smfmac_f32_32x32x32_bf16 v[232:247], v[152:155], v[200:207], v252\n" \
  "v_exp_f32 v210, v210\n" \
  "v_exp_f32 v211, v211\n" \
  "v_exp_f32 v212, v212\n" \
  "v_exp_f32 v213, v213\n" \
  "v_exp_f32 v214, v214\n" \
  "v_exp_f32 v215, v215\n" \
  "v_exp_f32 v216, v216\n" \
  "v_exp_f32 v217, v217\n" \
  "v_smfmac_f32_32x32x32_bf16 v[232:247], v[156:159], v[208:215], v252\n" \
  "v_exp_f32 v218, v218\n" \
  "v_exp_f32 v219, v219\n" \
  "v_exp_f32 v220, v220\n" \
  "v_mfma_f32_32x32x16_bf16 v[200:215], v[56:59], v[248:251], 0\n" \
  "v_exp_f32 v221, v221\n" \
  "v_exp_f32 v222, v222\n" \
  "v_exp_f32 v223, v223\n" \
  "v_exp_f32 v224, v224\n" \
  "v_exp_f32 v225, v225\n" \
  "v_smfmac_f32_32x32x32_bf16 v[232:247], v[160:163], v[216:223], v252\n" \
  "v_exp_f32 v226, v226\n" \
  "v_exp_f32 v227, v227\n" \
  "v_exp_f32 v228, v228\n" \
  "v_exp_f32 v229, v229\n" \
  "v_exp_f32 v230, v230\n" \
  "v_exp_f32 v231, v231\n" \
  "v_exp_f32 v200, v200\n" \
  "v_exp_f32 v201, v201\n" \
  "v_smfmac_f32_32x32x32_bf16 v[232:247], v[164:167], v[224:231], v252\n" \
  "v_exp_f32 v202, v202\n" \
  "v_exp_f32 v203, v203\n" \
  "v_exp_f32 v204, v204\n" \
  "v_mfma_f32_32x32x16_bf16 v[216:231], v[60:63], v[248:251], 0\n" \
  "v_exp_f32 v205, v205\n" \
  "v_exp_f32 v206, v206\n" \
  "v_exp_f32 v207, v207\n" \
  "v_exp_f32 v208, v208\n" \
  "v_exp_f32 v209, v209\n" \
  "v_smfmac_f32_32x32x32_bf16 v[232:247], v[168:171], v[200:207], v252\n" \
  "v_exp_f32 v210, v210\n" \
  "v_exp_f32 v211, v211\n" \
  "v_exp_f32 v212, v212\n" \
  "v_exp_f32 v213, v213\n" \
  "v_exp_f32 v214, v214\n" \
  "v_exp_f32 v215, v215\n" \
  "v_exp_f32 v216, v216\n" \
  "v_exp_f32 v217, v217\n" \
  "v_smfmac_f32_32x32x32_bf16 v[232:247], v[172:175], v[208:215], v252\n" \
  "v_exp_f32 v218, v218\n" \
  "v_exp_f32 v219, v219\n" \
  "v_exp_f32 v220, v220\n" \
  "v_mfma_f32_32x32x16_bf16 v[200:215], v[64:67], v[248:251], 0\n" \
  "v_exp_f32 v221, v221\n" \
  "v_exp_f32 v222, v222\n" \
  "v_exp_f32 v223, v223\n" \
  "v_exp_f32 v224, v224\n" \
  "v_exp_f32 v225, v225\n" \
  "v_smfmac_f32_32x32x32_bf16 v[232:247], v[176:179], v[216:223], v252\n" \
  "v_exp_f32 v226, v226\n" \
  "v_exp_f32 v227, v227\n" \
  "v_exp_f32 v228, v228\n" \
  "v_exp_f32 v229, v229\n" \
  "v_exp_f32 v230, v230\n" \
  "v_exp_f32 v231, v231\n" \
  "v_exp_f32 v200, v200\n" \
  "v_exp_f32 v201, v201\n" \
  "v_smfmac_f32_32x32x32_bf16 v[232:247], v[180:183], v[224:231], v252\n" \
  "v_exp_f32 v202, v202\n" \
  "v_exp_f32 v203, v203\n" \
  "v_exp_f32 v204, v204\n" \
  "v_mfma_f32_32x32x16_bf16 v[216:231], v[68:71], v[248:251], 0\n" \
  "ds_read_b128 v[248:251], v253 offset:14336\n" \
  "v_exp_f32 v205, v205\n" \
  "v_exp_f32 v206, v206\n" \
  "v_exp_f32 v207, v207\n" \
  "v_exp_f32 v208, v208\n" \
  "v_exp_f32 v209, v209\n" \
  "v_smfmac_f32_32x32x32_bf16 v[232:247], v[184:187], v[200:207], v252\n" \
  "v_exp_f32 v210, v210\n" \
  "v_exp_f32 v211, v211\n" \
  "v_exp_f32 v212, v212\n" \
  "v_exp_f32 v213, v213\n" \
  "v_exp_f32 v214, v214\n" \
  "v_exp_f32 v215, v215\n" \
  "v_exp_f32 v216, v216\n" \
  "v_exp_f32 v217, v217\n" \
  "v_smfmac_f32_32x32x32_bf16 v[232:247], v[188:191], v[208:215], v252\n" \
  "s_waitcnt lgkmcnt(0)\n" \
  "v_exp_f32 v218, v218\n" \
  "v_exp_f32 v219, v219\n" \
  "v_exp_f32 v220, v220\n" \
  "v_mfma_f32_32x32x16_bf16 v[200:215], v[8:11], v[248:251], 0\n" \
  "v_exp_f32 v221, v221\n" \
  "v_exp_f32 v222, v222\n" \
  "v_exp_f32 v223, v223\n" \
  "v_exp_f32 v224, v224\n" \
  "v_exp_f32 v225, v225\n" \
  "v_smfmac_f32_32x32x32_bf16 v[232:247], v[192:195], v[216:223], v252\n" \
  "v_exp_f32 v226, v226\n" \
  "v_exp_f32 v227, v227\n" \
  "v_exp_f32 v228, v228\n" \
  "v_exp_f32 v229, v229\n" \
  "v_exp_f32 v230, v230\n" \
  "v_exp_f32 v231, v231\n" \
  "v_exp_f32 v200, v200\n" \
  "v_exp_f32 v201, v201\n" \
  "v_smfmac_f32_32x32x32_bf16 v[232:247], v[196:199], v[224:231], v252\n" \
  "v_exp_f32 v202, v202\n" \
  "v_exp_f32 v203, v203\n" \
  "v_exp_f32 v204, v204\n" \
  "v_mfma_f32_32x32x16_bf16 v[216:231], v[12:15], v[248:251], 0\n" \
  "v_exp_f32 v205, v205\n" \
  "v_exp_f32 v206, v206\n" \
  "v_exp_f32 v207, v207\n" \
  "v_exp_f32 v208, v208\n" \
  "v_exp_f32 v209, v209\n" \
  "v_exp_f32 v210, v210\n" \
  "v_exp_f32 v211, v211\n" \
  "v_exp_f32 v212, v212\n" \
  "v_mov_b64 v[0:1], v[232:233]\n" \
  "v_mov_b64 v[2:3], v[234:235]\n" \
  "v_mov_b64 v[4:5], v[236:237]\n" \
  "v_mov_b64 v[6:7], v[238:239]\n" \
  "v_mov_b32 v255, v240\n" \
  "v_mov_b64 v[232:233], 0\n" \
  "v_mov_b64 v[234:235], 0\n" \
  "v_mov_b64 v[236:237], 0\n" \
  "v_mov_b64 v[238:239], 0\n" \
  "v_mov_b64 v[240:241], 0\n" \
  "v_exp_f32 v213, v213\n" \
  "v_exp_f32 v214, v214\n" \
  "v_smfmac_f32_32x32x32_bf16 v[232:247], v[72:75], v[200:207], v252\n" \
  "v_exp_f32 v215, v215\n" \
  "v_exp_f32 v216, v216\n" \
  "v_exp_f32 v217, v217\n" \
  "v_smfmac_f32_32x32x32_bf16 v[232:247], v[76:79], v[208:215], v252\n" \
  "ds_write_b128 v254, v[0:3] offset:28672\n" \
  "v_exp_f32 v218, v218\n" \
  "v_exp_f32 v219, v219\n" \
  "v_exp_f32 v220, v220\n" \
  "v_mfma_f32_32x32x16_bf16 v[200:215], v[16:19], v[248:251], 0\n" \
  "v_exp_f32 v221, v221\n" \
  "v_exp_f32 v222, v222\n" \
  "v_exp_f32 v223, v223\n" \
  "v_exp_f32 v224, v224\n" \
  "v_exp_f32 v225, v225\n" \
  "v_smfmac_f32_32x32x32_bf16 v[232:247], v[80:83], v[216:223], v252\n" \
  "ds_write_b128 v254, v[4:7] offset:29696\n" \
  "v_exp_f32 v226, v226\n" \
  "v_exp_f32 v227, v227\n" \
  "v_exp_f32 v228, v228\n" \
  "v_exp_f32 v229, v229\n" \
  "v_exp_f32 v230, v230\n" \
  "v_exp_f32 v231, v231\n" \
  "v_exp_f32 v200, v200\n" \
  "v_exp_f32 v201, v201\n" \
  "v_smfmac_f32_32x32x32_bf16 v[232:247], v[84:87], v[224:231], v252\n" \
  "ds_write_b32 v254, v255 offset:30720\n" \
  "v_exp_f32 v202, v202\n" \
  "v_exp_f32 v203, v203\n" \
  "v_exp_f32 v204, v204\n" \
  "v_mfma_f32_32x32x16_bf16 v[216:231], v[20:23], v[248:251], 0\n" \
  "v_exp_f32 v205, v205\n" \
  "v_exp_f32 v206, v206\n" \
  "v_exp_f32 v207, v207\n" \
  "v_exp_f32 v208, v208\n" \
  "v_exp_f32 v209, v209\n" \
  "v_smfmac_f32_32x32x32_bf16 v[232:247], v[88:91], v[200:207], v252\n" \
  "v_exp_f32 v210, v210\n" \
  "v_exp_f32 v211, v211\n" \
  "v_exp_f32 v212, v212\n" \
  "v_exp_f32 v213, v213\n" \
  "v_exp_f32 v214, v214\n" \
  "v_exp_f32 v215, v215\n" \
  "v_exp_f32 v216, v216\n" \
  "v_exp_f32 v217, v217\n" \
  "v_smfmac_f32_32x32x32_bf16 v[232:247], v[92:95], v[208:215], v252\n" \
  "v_exp_f32 v218, v218\n" \
  "v_exp_f32 v219, v219\n" \
  "v_exp_f32 v220, v220\n" \
  "v_mfma_f32_32x32x16_bf16 v[200:215], v[24:27], v[248:251], 0\n" \
  "v_exp_f32 v221, v221\n" \
  "v_exp_f32 v222, v222\n" \
  "v_exp_f32 v223, v223\n" \
  "v_exp_f32 v224, v224\n" \
  "v_exp_f32 v225, v225\n" \
  "v_smfmac_f32_32x32x32_bf16 v[232:247], v[96:99], v[216:223], v252\n" \
  "v_exp_f32 v226, v226\n" \
  "v_exp_f32 v227, v227\n" \
  "v_exp_f32 v228, v228\n" \
  "v_exp_f32 v229, v229\n" \
  "v_exp_f32 v230, v230\n" \
  "v_exp_f32 v231, v231\n" \
  "v_exp_f32 v200, v200\n" \
  "v_exp_f32 v201, v201\n" \
  "v_smfmac_f32_32x32x32_bf16 v[232:247], v[100:103], v[224:231], v252\n" \
  "v_exp_f32 v202, v202\n" \
  "v_exp_f32 v203, v203\n" \
  "v_exp_f32 v204, v204\n" \
  "v_mfma_f32_32x32x16_bf16 v[216:231], v[28:31], v[248:251], 0\n" \
  "v_exp_f32 v205, v205\n" \
  "v_exp_f32 v206, v206\n" \
  "v_exp_f32 v207, v207\n" \
  "v_exp_f32 v208, v208\n" \
  "v_exp_f32 v209, v209\n" \
  "v_smfmac_f32_32x32x32_bf16 v[232:247], v[104:107], v[200:207], v252\n" \
  "v_exp_f32 v210, v210\n" \
  "v_exp_f32 v211, v211\n" \
  "v_exp_f32 v212, v212\n" \
  "v_exp_f32 v213, v213\n" \
  "v_exp_f32 v214, v214\n" \
  "v_exp_f32 v215, v215\n" \
  "v_exp_f32 v216, v216\n" \
  "v_exp_f32 v217, v217\n" \
  "v_smfmac_f32_32x32x32_bf16 v[232:247], v[108:111], v[208:215], v252\n" \
  "v_exp_f32 v218, v218\n" \
  "v_exp_f32 v219, v219\n" \
  "v_exp_f32 v220, v220\n" \
  "v_mfma_f32_32x32x16_bf16 v[200:215], v[32:35], v[248:251], 0\n" \
  "v_exp_f32 v221, v221\n" \
  "v_exp_f32 v222, v222\n" \
  "v_exp_f32 v223, v223\n" \
  "v_exp_f32 v224, v224\n" \
  "v_exp_f32 v225, v225\n" \
  "v_smfmac_f32_32x32x32_bf16 v[232:247], v[112:115], v[216:223], v252\n" \
  "s_waitcnt lgkmcnt(0)\n" \
  "s_barrier\n" \
  "v_exp_f32 v226, v226\n" \
  "v_exp_f32 v227, v227\n" \
  "v_exp_f32 v228, v228\n" \
  "v_exp_f32 v229, v229\n" \
  "v_exp_f32 v230, v230\n" \
  "v_exp_f32 v231, v231\n" \
  "v_exp_f32 v200, v200\n" \
  "v_exp_f32 v201, v201\n" \
  "v_smfmac_f32_32x32x32_bf16 v[232:247], v[116:119], v[224:231], v252\n" \
  "v_exp_f32 v202, v202\n" \
  "v_exp_f32 v203, v203\n" \
  "v_exp_f32 v204, v204\n" \
  "v_mfma_f32_32x32x16_bf16 v[216:231], v[36:39], v[248:251], 0\n" \
  "v_exp_f32 v205, v205\n" \
  "v_exp_f32 v206, v206\n" \
  "v_exp_f32 v207, v207\n" \
  "v_exp_f32 v208, v208\n" \
  "v_exp_f32 v209, v209\n" \
  "v_smfmac_f32_32x32x32_bf16 v[232:247], v[120:123], v[200:207], v252\n" \
  "v_exp_f32 v210, v210\n" \
  "v_exp_f32 v211, v211\n" \
  "v_exp_f32 v212, v212\n" \
  "v_exp_f32 v213, v213\n" \
  "v_exp_f32 v214, v214\n" \
  "v_exp_f32 v215, v215\n" \
  "v_exp_f32 v216, v216\n" \
  "v_exp_f32 v217, v217\n" \
  "v_smfmac_f32_32x32x32_bf16 v[232:247], v[124:127], v[208:215], v252\n" \
  "v_exp_f32 v218, v218\n" \
  "v_exp_f32 v219, v219\n" \
  "v_exp_f32 v220, v220\n" \
  "v_mfma_f32_32x32x16_bf16 v[200:215], v[40:43], v[248:251], 0\n" \
  "v_exp_f32 v221, v221\n" \
  "v_exp_f32 v222, v222\n" \
  "v_exp_f32 v223, v223\n" \
  "v_exp_f32 v224, v224\n" \
  "v_exp_f32 v225, v225\n" \
  "v_smfmac_f32_32x32x32_bf16 v[232:247], v[128:131], v[216:223], v252\n" \
  "v_exp_f32 v226, v226\n" \
  "v_exp_f32 v227, v227\n" \
  "v_exp_f32 v228, v228\n" \
  "v_exp_f32 v229, v229\n" \
  "v_exp_f32 v230, v230\n" \
  "v_exp_f32 v231, v231\n" \
  "v_exp_f32 v200, v200\n" \
  "v_exp_f32 v201, v201\n" \
  "v_smfmac_f32_32x32x32_bf16 v[232:247], v[132:135], v[224:231], v252\n" \
  "v_exp_f32 v202, v202\n" \
  "v_exp_f32 v203, v203\n" \
  "v_exp_f32 v204, v204\n" \
  "v_mfma_f32_32x32x16_bf16 v[216:231], v[44:47], v[248:251], 0\n" \
  "v_exp_f32 v205, v205\n" \
  "v_exp_f32 v206, v206\n" \
  "v_exp_f32 v207, v207\n" \
  "v_exp_f32 v208, v208\n" \
  "v_exp_f32 v209, v209\n" \
  "v_smfmac_f32_32x32x32_bf16 v[232:247], v[136:139], v[200:207], v252\n" \
  "v_exp_f32 v210, v210\n" \
  "v_exp_f32 v211, v211\n" \
  "v_exp_f32 v212, v212\n" \
  "v_exp_f32 v213, v213\n" \
  "v_exp_f32 v214, v214\n" \
  "v_exp_f32 v215, v215\n" \
  "v_exp_f32 v216, v216\n" \
  "v_exp_f32 v217, v217\n" \
  "v_smfmac_f32_32x32x32_bf16 v[232:247], v[140:143], v[208:215], v252\n" \
  "v_exp_f32 v218, v218\n" \
  "v_exp_f32 v219, v219\n" \
  "v_exp_f32 v220, v220\n" \
  "v_mfma_f32_32x32x16_bf16 v[200:215], v[48:51], v[248:251], 0\n" \
  "v_exp_f32 v221, v221\n" \
  "v_exp_f32 v222, v222\n" \
  "v_exp_f32 v223, v223\n" \
  "v_exp_f32 v224, v224\n" \
  "v_exp_f32 v225, v225\n" \
  "v_smfmac_f32_32x32x32_bf16 v[232:247], v[144:147], v[216:223], v252\n" \
  "v_exp_f32 v226, v226\n" \
  "v_exp_f32 v227, v227\n" \
  "v_exp_f32 v228, v228\n" \
  "v_exp_f32 v229, v229\n" \
  "v_exp_f32 v230, v230\n" \
  "v_exp_f32 v231, v231\n" \
  "v_exp_f32 v200, v200\n" \
  "v_exp_f32 v201, v201\n" \
  "v_smfmac_f32_32x32x32_bf16 v[232:247], v[148:151], v[224:231], v252\n" \
  "v_exp_f32 v202, v202\n" \
  "v_exp_f32 v203, v203\n" \
  "v_exp_f32 v204, v204\n" \
  "v_mfma_f32_32x32x16_bf16 v[216:231], v[52:55], v[248:251], 0\n" \
  "v_exp_f32 v205, v205\n" \
  "v_exp_f32 v206, v206\n" \
  "v_exp_f32 v207, v207\n" \
  "v_exp_f32 v208, v208\n" \
  "v_exp_f32 v209, v209\n" \
  "v_smfmac_f32_32x32x32_bf16 v[232:247], v[152:155], v[200:207], v252\n" \
  "v_exp_f32 v210, v210\n" \
  "v_exp_f32 v211, v211\n" \
  "v_exp_f32 v212, v212\n" \
  "v_exp_f32 v213, v213\n" \
  "v_exp_f32 v214, v214\n" \
  "v_exp_f32 v215, v215\n" \
  "v_exp_f32 v216, v216\n" \
  "v_exp_f32 v217, v217\n" \
  "v_smfmac_f32_32x32x32_bf16 v[232:247], v[156:159], v[208:215], v252\n" \
  "v_exp_f32 v218, v218\n" \
  "v_exp_f32 v219, v219\n" \
  "v_exp_f32 v220, v220\n" \
  "v_mfma_f32_32x32x16_bf16 v[200:215], v[56:59], v[248:251], 0\n" \
  "v_exp_f32 v221, v221\n" \
  "v_exp_f32 v222, v222\n" \
  "v_exp_f32 v223, v223\n" \
  "v_exp_f32 v224, v224\n" \
  "v_exp_f32 v225, v225\n" \
  "v_smfmac_f32_32x32x32_bf16 v[232:247], v[160:163], v[216:223], v252\n" \
  "v_exp_f32 v226, v226\n" \
  "v_exp_f32 v227, v227\n" \
  "v_exp_f32 v228, v228\n" \
  "v_exp_f32 v229, v229\n" \
  "v_exp_f32 v230, v230\n" \
  "v_exp_f32 v231, v231\n" \
  "v_exp_f32 v200, v200\n" \
  "v_exp_f32 v201, v201\n" \
  "v_smfmac_f32_32x32x32_bf16 v[232:247], v[164:167], v[224:231], v252\n" \
  "v_exp_f32 v202, v202\n" \
  "v_exp_f32 v203, v203\n" \
  "v_exp_f32 v204, v204\n" \
  "v_mfma_f32_32x32x16_bf16 v[216:231], v[60:63], v[248:251], 0\n" \
  "v_exp_f32 v205, v205\n" \
  "v_exp_f32 v206, v206\n" \
  "v_exp_f32 v207, v207\n" \
  "v_exp_f32 v208, v208\n" \
  "v_exp_f32 v209, v209\n" \
  "v_smfmac_f32_32x32x32_bf16 v[232:247], v[168:171], v[200:207], v252\n" \
  "v_exp_f32 v210, v210\n" \
  "v_exp_f32 v211, v211\n" \
  "v_exp_f32 v212, v212\n" \
  "v_exp_f32 v213, v213\n" \
  "v_exp_f32 v214, v214\n" \
  "v_exp_f32 v215, v215\n" \
  "v_exp_f32 v216, v216\n" \
  "v_exp_f32 v217, v217\n" \
  "v_smfmac_f32_32x32x32_bf16 v[232:247], v[172:175], v[208:215], v252\n" \
  "v_exp_f32 v218, v218\n" \
  "v_exp_f32 v219, v219\n" \
  "v_exp_f32 v220, v220\n" \
  "v_mfma_f32_32x32x16_bf16 v[200:215], v[64:67], v[248:251], 0\n" \
  "v_exp_f32 v221, v221\n" \
  "v_exp_f32 v222, v222\n" \
  "v_exp_f32 v223, v223\n" \
  "v_exp_f32 v224, v224\n" \
  "v_exp_f32 v225, v225\n" \
  "v_smfmac_f32_32x32x32_bf16 v[232:247], v[176:179], v[216:223], v252\n" \
  "v_exp_f32 v226, v226\n" \
  "v_exp_f32 v227, v227\n" \
  "v_exp_f32 v228, v228\n" \
  "v_exp_f32 v229, v229\n" \
  "v_exp_f32 v230, v230\n" \
  "v_exp_f32 v231, v231\n" \
  "v_exp_f32 v200, v200\n" \
  "v_exp_f32 v201, v201\n" \
  "v_smfmac_f32_32x32x32_bf16 v[232:247], v[180:183], v[224:231], v252\n" \
  "v_exp_f32 v202, v202\n" \
  "v_exp_f32 v203, v203\n" \
  "v_exp_f32 v204, v204\n" \
  "v_mfma_f32_32x32x16_bf16 v[216:231], v[68:71], v[248:251], 0\n" \
  "ds_read_b128 v[248:251], v253 offset:15360\n" \
  "v_exp_f32 v205, v205\n" \
  "v_exp_f32 v206, v206\n" \
  "v_exp_f32 v207, v207\n" \
  "v_exp_f32 v208, v208\n" \
  "v_exp_f32 v209, v209\n" \
  "v_smfmac_f32_32x32x32_bf16 v[232:247], v[184:187], v[200:207], v252\n" \
  "v_exp_f32 v210, v210\n" \
  "v_exp_f32 v211, v211\n" \
  "v_exp_f32 v212, v212\n" \
  "v_exp_f32 v213, v213\n" \
  "v_exp_f32 v214, v214\n" \
  "v_exp_f32 v215, v215\n" \
  "v_exp_f32 v216, v216\n" \
  "v_exp_f32 v217, v217\n" \
  "v_smfmac_f32_32x32x32_bf16 v[232:247], v[188:191], v[208:215], v252\n" \
  "s_waitcnt lgkmcnt(0)\n" \
  "v_exp_f32 v218, v218\n" \
  "v_exp_f32 v219, v219\n" \
  "v_exp_f32 v220, v220\n" \
  "v_mfma_f32_32x32x16_bf16 v[200:215], v[8:11], v[248:251], 0\n" \
  "v_exp_f32 v221, v221\n" \
  "v_exp_f32 v222, v222\n" \
  "v_exp_f32 v223, v223\n" \
  "v_exp_f32 v224, v224\n" \
  "v_exp_f32 v225, v225\n" \
  "v_smfmac_f32_32x32x32_bf16 v[232:247], v[192:195], v[216:223], v252\n" \
  "v_exp_f32 v226, v226\n" \
  "v_exp_f32 v227, v227\n" \
  "v_exp_f32 v228, v228\n" \
  "v_exp_f32 v229, v229\n" \
  "v_exp_f32 v230, v230\n" \
  "v_exp_f32 v231, v231\n" \
  "v_exp_f32 v200, v200\n" \
  "v_exp_f32 v201, v201\n" \
  "v_smfmac_f32_32x32x32_bf16 v[232:247], v[196:199], v[224:231], v252\n" \
  "v_exp_f32 v202, v202\n" \
  "v_exp_f32 v203, v203\n" \
  "v_exp_f32 v204, v204\n" \
  "v_mfma_f32_32x32x16_bf16 v[216:231], v[12:15], v[248:251], 0\n" \
  "v_exp_f32 v205, v205\n" \
  "v_exp_f32 v206, v206\n" \
  "v_exp_f32 v207, v207\n" \
  "v_exp_f32 v208, v208\n" \
  "v_exp_f32 v209, v209\n" \
  "v_exp_f32 v210, v210\n" \
  "v_exp_f32 v211, v211\n" \
  "v_exp_f32 v212, v212\n" \
  "v_mov_b64 v[0:1], v[232:233]\n" \
  "v_mov_b64 v[2:3], v[234:235]\n" \
  "v_mov_b64 v[4:5], v[236:237]\n" \
  "v_mov_b64 v[6:7], v[238:239]\n" \
  "v_mov_b32 v255, v240\n" \
  "v_mov_b64 v[232:233], 0\n" \
  "v_mov_b64 v[234:235], 0\n" \
  "v_mov_b64 v[236:237], 0\n" \
  "v_mov_b64 v[238:239], 0\n" \
  "v_mov_b64 v[240:241], 0\n" \
  "v_exp_f32 v213, v213\n" \
  "v_exp_f32 v214, v214\n" \
  "v_smfmac_f32_32x32x32_bf16 v[232:247], v[72:75], v[200:207], v252\n" \
  "v_exp_f32 v215, v215\n" \
  "v_exp_f32 v216, v216\n" \
  "v_exp_f32 v217, v217\n" \
  "v_smfmac_f32_32x32x32_bf16 v[232:247], v[76:79], v[208:215], v252\n" \
  "ds_write_b128 v254, v[0:3] offset:16384\n" \
  "v_exp_f32 v218, v218\n" \
  "v_exp_f32 v219, v219\n" \
  "v_exp_f32 v220, v220\n" \
  "v_mfma_f32_32x32x16_bf16 v[200:215], v[16:19], v[248:251], 0\n" \
  "v_exp_f32 v221, v221\n" \
  "v_exp_f32 v222, v222\n" \
  "v_exp_f32 v223, v223\n" \
  "v_exp_f32 v224, v224\n" \
  "v_exp_f32 v225, v225\n" \
  "v_smfmac_f32_32x32x32_bf16 v[232:247], v[80:83], v[216:223], v252\n" \
  "ds_write_b128 v254, v[4:7] offset:17408\n" \
  "v_exp_f32 v226, v226\n" \
  "v_exp_f32 v227, v227\n" \
  "v_exp_f32 v228, v228\n" \
  "v_exp_f32 v229, v229\n" \
  "v_exp_f32 v230, v230\n" \
  "v_exp_f32 v231, v231\n" \
  "v_exp_f32 v200, v200\n" \
  "v_exp_f32 v201, v201\n" \
  "v_smfmac_f32_32x32x32_bf16 v[232:247], v[84:87], v[224:231], v252\n" \
  "ds_write_b32 v254, v255 offset:18432\n" \
  "v_exp_f32 v202, v202\n" \
  "v_exp_f32 v203, v203\n" \
  "v_exp_f32 v204, v204\n" \
  "v_mfma_f32_32x32x16_bf16 v[216:231], v[20:23], v[248:251], 0\n" \
  "v_exp_f32 v205, v205\n" \
  "v_exp_f32 v206, v206\n" \
  "v_exp_f32 v207, v207\n" \
  "v_exp_f32 v208, v208\n" \
  "v_exp_f32 v209, v209\n" \
  "v_smfmac_f32_32x32x32_bf16 v[232:247], v[88:91], v[200:207], v252\n" \
  "v_exp_f32 v210, v210\n" \
  "v_exp_f32 v211, v211\n" \
  "v_exp_f32 v212, v212\n" \
  "v_exp_f32 v213, v213\n" \
  "v_exp_f32 v214, v214\n" \
  "v_exp_f32 v215, v215\n" \
  "v_exp_f32 v216, v216\n" \
  "v_exp_f32 v217, v217\n" \
  "v_smfmac_f32_32x32x32_bf16 v[232:247], v[92:95], v[208:215], v252\n" \
  "v_exp_f32 v218, v218\n" \
  "v_exp_f32 v219, v219\n" \
  "v_exp_f32 v220, v220\n" \
  "v_mfma_f32_32x32x16_bf16 v[200:215], v[24:27], v[248:251], 0\n" \
  "v_exp_f32 v221, v221\n" \
  "v_exp_f32 v222, v222\n" \
  "v_exp_f32 v223, v223\n" \
  "v_exp_f32 v224, v224\n" \
  "v_exp_f32 v225, v225\n" \
  "v_smfmac_f32_32x32x32_bf16 v[232:247], v[96:99], v[216:223], v252\n" \
  "s_waitcnt lgkmcnt(0)\n" \
  "s_barrier\n" \
  "v_exp_f32 v226, v226\n" \
  "v_exp_f32 v227, v227\n" \
  "v_exp_f32 v228, v228\n" \
  "v_exp_f32 v229, v229\n" \
  "v_exp_f32 v230, v230\n" \
  "v_exp_f32 v231, v231\n" \
  "v_exp_f32 v200, v200\n" \
  "v_exp_f32 v201, v201\n" \
  "v_smfmac_f32_32x32x32_bf16 v[232:247], v[100:103], v[224:231], v252\n" \
  "v_exp_f32 v202, v202\n" \
  "v_exp_f32 v203, v203\n" \
  "v_exp_f32 v204, v204\n" \
  "v_mfma_f32_32x32x16_bf16 v[216:231], v[28:31], v[248:251], 0\n" \
  "v_exp_f32 v205, v205\n" \
  "v_exp_f32 v206, v206\n" \
  "v_exp_f32 v207, v207\n" \
  "v_exp_f32 v208, v208\n" \
  "v_exp_f32 v209, v209\n" \
  "v_smfmac_f32_32x32x32_bf16 v[232:247], v[104:107], v[200:207], v252\n" \
  "v_exp_f32 v210, v210\n" \
  "v_exp_f32 v211, v211\n" \
  "v_exp_f32 v212, v212\n" \
  "v_exp_f32 v213, v213\n" \
  "v_exp_f32 v214, v214\n" \
  "v_exp_f32 v215, v215\n" \
  "v_exp_f32 v216, v216\n" \
  "v_exp_f32 v217, v217\n" \
  "v_smfmac_f32_32x32x32_bf16 v[232:247], v[108:111], v[208:215], v252\n" \
  "v_exp_f32 v218, v218\n" \
  "v_exp_f32 v219, v219\n" \
  "v_exp_f32 v220, v220\n" \
  "v_mfma_f32_32x32x16_bf16 v[200:215], v[32:35], v[248:251], 0\n" \
  "v_exp_f32 v221, v221\n" \
  "v_exp_f32 v222, v222\n" \
  "v_exp_f32 v223, v223\n" \
  "v_exp_f32 v224, v224\n" \
  "v_exp_f32 v225, v225\n" \
  "v_smfmac_f32_32x32x32_bf16 v[232:247], v[112:115], v[216:223], v252\n" \
  "v_exp_f32 v226, v226\n" \
  "v_exp_f32 v227, v227\n" \
  "v_exp_f32 v228, v228\n" \
  "v_exp_f32 v229, v229\n" \
  "v_exp_f32 v230, v230\n" \
  "v_exp_f32 v231, v231\n" \
  "v_exp_f32 v200, v200\n" \
  "v_exp_f32 v201, v201\n" \
  "v_smfmac_f32_32x32x32_bf16 v[232:247], v[116:119], v[224:231], v252\n" \
  "v_exp_f32 v202, v202\n" \
  "v_exp_f32 v203, v203\n" \
  "v_exp_f32 v204, v204\n" \
  "v_mfma_f32_32x32x16_bf16 v[216:231], v[36:39], v[248:251], 0\n" \
  "v_exp_f32 v205, v205\n" \
  "v_exp_f32 v206, v206\n" \
  "v_exp_f32 v207, v207\n" \
  "v_exp_f32 v208, v208\n" \
  "v_exp_f32 v209, v209\n" \
  "v_smfmac_f32_32x32x32_bf16 v[232:247], v[120:123], v[200:207], v252\n" \
  "v_exp_f32 v210, v210\n" \
  "v_exp_f32 v211, v211\n" \
  "v_exp_f32 v212, v212\n" \
  "v_exp_f32 v213, v213\n" \
  "v_exp_f32 v214, v214\n" \
  "v_exp_f32 v215, v215\n" \
  "v_exp_f32 v216, v216\n" \
  "v_exp_f32 v217, v217\n" \
  "v_smfmac_f32_32x32x32_bf16 v[232:247], v[124:127], v[208:215], v252\n" \
  "v_exp_f32 v218, v218\n" \
  "v_exp_f32 v219, v219\n" \
  "v_exp_f32 v220, v220\n" \
  "v_mfma_f32_32x32x16_bf16 v[200:215], v[40:43], v[248:251], 0\n" \
  "v_exp_f32 v221, v221\n" \
  "v_exp_f32 v222, v222\n" \
  "v_exp_f32 v223, v223\n" \
  "v_exp_f32 v224, v224\n" \
  "v_exp_f32 v225, v225\n" \
  "v_smfmac_f32_32x32x32_bf16 v[232:247], v[128:131], v[216:223], v252\n" \
  "v_exp_f32 v226, v226\n" \
  "v_exp_f32 v227, v227\n" \
  "v_exp_f32 v228, v228\n" \
  "v_exp_f32 v229, v229\n" \
  "v_exp_f32 v230, v230\n" \
  "v_exp_f32 v231, v231\n" \
  "v_exp_f32 v200, v200\n" \
  "v_exp_f32 v201, v201\n" \
  "v_smfmac_f32_32x32x32_bf16 v[232:247], v[132:135], v[224:231], v252\n" \
  "v_exp_f32 v202, v202\n" \
  "v_exp_f32 v203, v203\n" \
  "v_exp_f32 v204, v204\n" \
  "v_mfma_f32_32x32x16_bf16 v[216:231], v[44:47], v[248:251], 0\n" \
  "v_exp_f32 v205, v205\n" \
  "v_exp_f32 v206, v206\n" \
  "v_exp_f32 v207, v207\n" \
  "v_exp_f32 v208, v208\n" \
  "v_exp_f32 v209, v209\n" \
  "v_smfmac_f32_32x32x32_bf16 v[232:247], v[136:139], v[200:207], v252\n" \
  "v_exp_f32 v210, v210\n" \
  "v_exp_f32 v211, v211\n" \
  "v_exp_f32 v212, v212\n" \
  "v_exp_f32 v213, v213\n" \
  "v_exp_f32 v214, v214\n" \
  "v_exp_f32 v215, v215\n" \
  "v_exp_f32 v216, v216\n" \
  "v_exp_f32 v217, v217\n" \
  "v_smfmac_f32_32x32x32_bf16 v[232:247], v[140:143], v[208:215], v252\n" \
  "v_exp_f32 v218, v218\n" \
  "v_exp_f32 v219, v219\n" \
  "v_exp_f32 v220, v220\n" \
  "v_mfma_f32_32x32x16_bf16 v[200:215], v[48:51], v[248:251], 0\n" \
  "v_exp_f32 v221, v221\n" \
  "v_exp_f32 v222, v222\n" \
  "v_exp_f32 v223, v223\n" \
  "v_exp_f32 v224, v224\n" \
  "v_exp_f32 v225, v225\n" \
  "v_smfmac_f32_32x32x32_bf16 v[232:247], v[144:147], v[216:223], v252\n" \
  ".Lpad3_%=:\n" \
  "s_cbranch_execz .Lmqa_finpad\n" \
  "v_exp_f32 v226, v226\n" \
  "v_exp_f32 v227, v227\n" \
  "v_exp_f32 v228, v228\n" \
  "v_exp_f32 v229, v229\n" \
  "v_exp_f32 v230, v230\n" \
  "v_exp_f32 v231, v231\n" \
  "v_exp_f32 v200, v200\n" \
  "v_exp_f32 v201, v201\n" \
  "v_smfmac_f32_32x32x32_bf16 v[232:247], v[148:151], v[224:231], v252\n" \
  "v_exp_f32 v202, v202\n" \
  "v_exp_f32 v203, v203\n" \
  "v_exp_f32 v204, v204\n" \
  "v_mfma_f32_32x32x16_bf16 v[216:231], v[52:55], v[248:251], 0\n" \
  "v_exp_f32 v205, v205\n" \
  "v_exp_f32 v206, v206\n" \
  "v_exp_f32 v207, v207\n" \
  "v_exp_f32 v208, v208\n" \
  "v_exp_f32 v209, v209\n" \
  "v_smfmac_f32_32x32x32_bf16 v[232:247], v[152:155], v[200:207], v252\n" \
  "v_exp_f32 v210, v210\n" \
  "v_exp_f32 v211, v211\n" \
  "v_exp_f32 v212, v212\n" \
  "v_exp_f32 v213, v213\n" \
  "v_exp_f32 v214, v214\n" \
  "v_exp_f32 v215, v215\n" \
  "v_exp_f32 v216, v216\n" \
  "v_exp_f32 v217, v217\n" \
  "v_smfmac_f32_32x32x32_bf16 v[232:247], v[156:159], v[208:215], v252\n" \
  "v_exp_f32 v218, v218\n" \
  "v_exp_f32 v219, v219\n" \
  "v_exp_f32 v220, v220\n" \
  "v_mfma_f32_32x32x16_bf16 v[200:215], v[56:59], v[248:251], 0\n" \
  "v_exp_f32 v221, v221\n" \
  "v_exp_f32 v222, v222\n" \
  "v_exp_f32 v223, v223\n" \
  "v_exp_f32 v224, v224\n" \
  "v_exp_f32 v225, v225\n" \
  "v_smfmac_f32_32x32x32_bf16 v[232:247], v[160:163], v[216:223], v252\n" \
  "v_exp_f32 v226, v226\n" \
  "v_exp_f32 v227, v227\n" \
  "v_exp_f32 v228, v228\n" \
  "v_exp_f32 v229, v229\n" \
  "v_exp_f32 v230, v230\n" \
  "v_exp_f32 v231, v231\n" \
  "v_exp_f32 v200, v200\n" \
  "v_exp_f32 v201, v201\n" \
  "v_smfmac_f32_32x32x32_bf16 v[232:247], v[164:167], v[224:231], v252\n" \
  "v_exp_f32 v202, v202\n" \
  "v_exp_f32 v203, v203\n" \
  "v_exp_f32 v204, v204\n" \
  "v_mfma_f32_32x32x16_bf16 v[216:231], v[60:63], v[248:251], 0\n" \
  "v_exp_f32 v205, v205\n" \
  "v_exp_f32 v206, v206\n" \
  "v_exp_f32 v207, v207\n" \
  "v_exp_f32 v208, v208\n" \
  "v_exp_f32 v209, v209\n" \
  "v_smfmac_f32_32x32x32_bf16 v[232:247], v[168:171], v[200:207], v252\n" \
  "v_exp_f32 v210, v210\n" \
  "v_exp_f32 v211, v211\n" \
  "v_exp_f32 v212, v212\n" \
  "v_exp_f32 v213, v213\n" \
  "v_exp_f32 v214, v214\n" \
  "v_exp_f32 v215, v215\n" \
  "v_exp_f32 v216, v216\n" \
  "v_exp_f32 v217, v217\n" \
  "v_smfmac_f32_32x32x32_bf16 v[232:247], v[172:175], v[208:215], v252\n" \
  "v_exp_f32 v218, v218\n" \
  "v_exp_f32 v219, v219\n" \
  "v_exp_f32 v220, v220\n" \
  "v_mfma_f32_32x32x16_bf16 v[200:215], v[64:67], v[248:251], 0\n" \
  "v_exp_f32 v221, v221\n" \
  "v_exp_f32 v222, v222\n" \
  "v_exp_f32 v223, v223\n" \
  "v_exp_f32 v224, v224\n" \
  "v_exp_f32 v225, v225\n" \
  "v_smfmac_f32_32x32x32_bf16 v[232:247], v[176:179], v[216:223], v252\n" \
  "v_exp_f32 v226, v226\n" \
  "v_exp_f32 v227, v227\n" \
  "v_exp_f32 v228, v228\n" \
  "v_exp_f32 v229, v229\n" \
  "v_exp_f32 v230, v230\n" \
  "v_exp_f32 v231, v231\n" \
  "v_exp_f32 v200, v200\n" \
  "v_exp_f32 v201, v201\n" \
  "v_smfmac_f32_32x32x32_bf16 v[232:247], v[180:183], v[224:231], v252\n" \
  "v_exp_f32 v202, v202\n" \
  "v_exp_f32 v203, v203\n" \
  "v_exp_f32 v204, v204\n" \
  "v_mfma_f32_32x32x16_bf16 v[216:231], v[68:71], v[248:251], 0\n" \
  "v_exp_f32 v205, v205\n" \
  "v_exp_f32 v206, v206\n" \
  "v_exp_f32 v207, v207\n" \
  "v_exp_f32 v208, v208\n" \
  "v_exp_f32 v209, v209\n" \
  "v_smfmac_f32_32x32x32_bf16 v[232:247], v[184:187], v[200:207], v252\n" \
  "v_exp_f32 v210, v210\n" \
  "v_exp_f32 v211, v211\n" \
  "v_exp_f32 v212, v212\n" \
  "v_exp_f32 v213, v213\n" \
  "v_exp_f32 v214, v214\n" \
  "v_exp_f32 v215, v215\n" \
  "v_exp_f32 v216, v216\n" \
  "v_exp_f32 v217, v217\n" \
  "v_smfmac_f32_32x32x32_bf16 v[232:247], v[188:191], v[208:215], v252\n" \
  "v_exp_f32 v218, v218\n" \
  "v_exp_f32 v219, v219\n" \
  "v_exp_f32 v220, v220\n" \
  "v_exp_f32 v221, v221\n" \
  "v_exp_f32 v222, v222\n" \
  "v_exp_f32 v223, v223\n" \
  "v_exp_f32 v224, v224\n" \
  "v_exp_f32 v225, v225\n" \
  "v_smfmac_f32_32x32x32_bf16 v[232:247], v[192:195], v[216:223], v252\n" \
  "v_exp_f32 v226, v226\n" \
  "v_exp_f32 v227, v227\n" \
  "v_exp_f32 v228, v228\n" \
  "v_exp_f32 v229, v229\n" \
  "v_exp_f32 v230, v230\n" \
  "v_exp_f32 v231, v231\n" \
  "s_nop 1\n" \
  "v_smfmac_f32_32x32x32_bf16 v[232:247], v[196:199], v[224:231], v252\n" \
  "s_nop 11\n" \
  "ds_write_b128 v254, v[232:235] offset:28672\n" \
  "ds_write_b128 v254, v[236:239] offset:29696\n" \
  "ds_write_b32 v254, v240 offset:30720\n" \
  "s_waitcnt lgkmcnt(0)\n" \
  "s_barrier\n" \
  "s_sleep 4\n" \
  "v_add_u32 v0, %[xso], v253\n" \
  "ds_read_b128 v[212:215], v0\n" \
  "ds_read_b128 v[216:219], v0 offset:1024\n" \
  "ds_read_b128 v[220:223], v0 offset:2048\n" \
  "v_add_u32 v1, 0x15000, v253\n" \
  "ds_read_b128 v[192:195], v1 offset:0\n" \
  "ds_read_b128 v[196:199], v1 offset:1024\n" \
  "ds_read_b128 v[200:203], v1 offset:2048\n" \
  "ds_read_b128 v[204:207], v1 offset:3072\n" \
  "ds_read_b128 v[208:211], v1 offset:4096\n" \
  "s_waitcnt lgkmcnt(0)\n" \
  ".Lend_%=:\n" \
  "s_barrier\n" \
  "ds_read_b128 v[224:227], v223\n" \
  "ds_read_b128 v[228:231], v223 offset:1088\n" \
  "v_mov_b32 v232, 91136\n" \
  "ds_read_b32 v232, v232\n" \
  "v_mov_b64 v[8:9], v[192:193]\n" \
  "v_mov_b64 v[10:11], v[194:195]\n" \
  "v_mov_b64 v[12:13], v[196:197]\n" \
  "v_mov_b64 v[14:15], v[198:199]\n" \
  "v_mov_b64 v[16:17], v[200:201]\n" \
  "v_mov_b64 v[18:19], v[202:203]\n" \
  "v_mov_b64 v[20:21], v[204:205]\n" \
  "v_mov_b64 v[22:23], v[206:207]\n" \
  "v_mbcnt_lo_u32_b32 v0, -1, 0\n" \
  "v_mbcnt_hi_u32_b32 v0, -1, v0\n" \
  "s_waitcnt lgkmcnt(0)\n"
#define ATT_CLOB "v24","v25","v26","v27","v28","v29","v30","v31","v32","v33","v34","v35","v36","v37","v38","v39","v40","v41","v42","v43","v44","v45","v46","v47","v48","v49","v50","v51","v52","v53","v54","v55","v56","v57","v58","v59","v60","v61","v62","v63","v64","v65","v66","v67","v68","v69","v70","v71","v72","v73","v74","v75","v76","v77","v78","v79","v80","v81","v82","v83","v84","v85","v86","v87","v88","v89","v90","v91","v92","v93","v94","v95","v96","v97","v98","v99","v100","v101","v102","v103","v104","v105","v106","v107","v108","v109","v110","v111","v112","v113","v114","v115","v116","v117","v118","v119","v120","v121","v122","v123","v124","v125","v126","v127","vcc","scc","memory"
#define ATT_RING(pf) "+{v[128:135]}"(pf[0]), "+{v[136:143]}"(pf[1]), "+{v[144:151]}"(pf[2]), "+{v[152:159]}"(pf[3]), "+{v[160:167]}"(pf[4]), "+{v[168:175]}"(pf[5]), "+{v[176:183]}"(pf[6]), "+{v[184:191]}"(pf[7]), "+{v[192:199]}"(pf[8]), "+{v[200:207]}"(pf[9]), "+{v[208:215]}"(pf[10]), "+{v[216:223]}"(pf[11]), "+{v[224:231]}"(pf[12]), "+{v[232:239]}"(pf[13]), "+{v[240:247]}"(pf[14]), "+{v[248:255]}"(pf[15])
#define ATT_OUT(a, b, c, d) "={v[8:11]}"(a), "={v[12:15]}"(b), "={v[16:19]}"(c), "={v[20:23]}"(d)
#define ATT_IN(x) "+{v0}"(x[0]), "+{v1}"(x[1]), "+{v2}"(x[2]), "+{v3}"(x[3]), "+{v4}"(x[4]), "+{v5}"(x[5]), "+{v6}"(x[6]), "+{v7}"(x[7])
#define ATT_NST 0
#define ATT_STOUT
DEVI void rows_write(float* rows, int l, int w, const f32x4* a) {
  int c16 = l & 15, q = l >> 4;
  f32x4 t0 = {a[0][0] + a[0][1], a[1][0] + a[1][1], a[2][0] + a[2][1], a[3][0] + a[3][1]};
  f32x4 t1 = {a[0][2] + a[0][3], a[1][2] + a[1][3], a[2][2] + a[2][3], a[3][2] + a[3][3]};
  int off = (c16 & 1) * 272 + (w * 8 + (c16 >> 1)) * 4;
  *(f32x4*)(rows + (2 * q) * 544 + off) = t0;
  *(f32x4*)(rows + (2 * q + 1) * 544 + off) = t1;
}

template <int N> DEVI void delay() { if constexpr (N > 0) __builtin_amdgcn_s_sleep(N); }
template <int I0, int I1> DEVI void prefetch(u32x8 (&pf)[16], bool on, rsrc_t rs, int l, unsigned base) {
  if (on) {
#pragma unroll
    for (int i = I0; i < I1; ++i) pf[i] = ldtile(rs, l * 16 + (i & 1) * 2048, base + (i >> 1) * 4096);
  }
}

template <int I0, int I1> DEVI void undef_tiles(u32x8 (&pf)[16]) {
#pragma unroll
  for (int i = I0; i < I1; ++i) UNDEF(pf[i]);
}
#define FRESH() do { w = w0; p = p0; asm volatile("v_mbcnt_lo_u32_b32 %0, -1, 0\n\tv_mbcnt_hi_u32_b32 %0, -1, %0" : "=v"(l)); __builtin_assume(l >= 0 && l < 64); asm volatile("" : "+s"(w)); asm volatile("" : "+s"(p)); t = w * 64 + l; c16 = l & 15; q = l >> 4; s2 = s2_of_pidx(p); } while (0)
#define FENCE_S() do { asm volatile("" : "+s"(w)); asm volatile("" : "+s"(p)); SB(); } while (0)
#define ROWOFF ((unsigned)(s2 + 256 * w) * 256 + c16 + 64 * q)
struct Ptrs { const unsigned* x; const u16 *wr, *wi, *wq, *wk, *wv, *wo; char* ws; };

struct AddrA { int aw, ar1, ar2, t16; };
DEVI AddrA addr_a(int w, int l) {
  const int c16 = l & 15, q = l >> 4, sg = 16 * w + c16;
  AddrA a = {(L_PART + (c16 + 64 * q) * 10 + w) * 4, (L_PART + sg * 10 + 2 * q) * 4, (L_PART + sel(sg != 0, 256 - sg, 128) * 10 + 2 * q) * 4, (w * 64 + l) * 16};
  PIN(a.aw); PIN(a.ar1); PIN(a.ar2); PIN(a.t16);
  return a;
}
template <int LAYER>
DEVI void spectral(const Ptrs& g, const rsrc_t rs, unsigned* lds, const int w0, const int p0, f32x4& re, f32x4& im, float& pv, Dft F, Tw TF, u32x4 mm, u32x8 (&pf)[16],
                   unsigned tc, AddrA ad STAMP_PARAMS) {
  int t = 0, w = w0, p = p0, l, c16, q, s2;
  if constexpr (LAYER == 0) FRESH();
  const f32x4 z4 = {0, 0, 0, 0};
  unsigned tag_lo = 0, tag_hi = 0;
  u32x2 fr, fi; u32x4 tv; unsigned sr, si, tvo, tdst, trb;
  if constexpr (LAYER == 0) {
    u32x4 xb;
    unsigned long long ep;
    asm volatile("s_load_dwordx2 %0, %1, 0x0" : "=s"(ep) : "s"(g.ws + OFF_EPO));
#pragma unroll
    for (int r = 0; r < 4; ++r) xb[r] = __builtin_amdgcn_raw_buffer_load_b32(__builtin_amdgcn_make_buffer_rsrc((void*)g.x, 0, 2 * MB, 0x00020000), (ROWOFF + 16 * r) * 4, 0, 0);
    SB();
    u32x4 sent = mk_sent();
    st128(sent, rs, t * 16, OFF_U + p * 8192);
    st128(sent, rs, t * 16, OFF_WR + p * 8192);
    if (w == 6) st128(sent, rs, sel(l < 2, l * 16, 0x7f000000), OFF_WS + p * 32);
    FENCE_S();
    const rsrc_t rwr = __builtin_amdgcn_make_buffer_rsrc((void*)g.wr, 0, 2048 * 257 * 2, 0x00020000);
    const rsrc_t rwi = __builtin_amdgcn_make_buffer_rsrc((void*)g.wi, 0, 2048 * 257 * 2, 0x00020000);
    const unsigned ro = (unsigned)((8 * s2 + kappa((w & 6) + (l & 1))) & 2047) * 514u, i4 = (t & 126) * 2;
    unsigned oa = ro + i4, ob = ro + 510 - i4, oc = sel((t & 126) == 0, ro + 256, 0x7f000000u);
    const int job = w >> 1, half = w & 1, sa = p >> 5;
    const bool isO = job == 1, kvt = job == 2, tile_on = (w < 4) | (kvt & (p < 16));
    const int ks = sel(kvt, p, p >> 2) & 7;
    const u16* src = sel(kvt, sel(p < 8, g.wk, g.wv), sel(isO, g.wo, g.wq));
    const rsrc_t rw = __builtin_amdgcn_make_buffer_rsrc((void*)src, 0, sel(tile_on, sel(kvt, 16384, 524288), 0), 0x00020000);
    const int rq = 128 * (ks >> 1) + 16 * (ks & 1) + 8 * half;
    const int so = sel(kvt, rq * 32, sel(isO, (sa * 64 + 4 * half + (p & 2) * 16 + (p & 1) * 8) * 1024 + ks * 128,
                                              (rq + (p & 2) * 2 + (p & 1) * 64) * 1024 + sa * 128));
    const int rowK = ((l >> 3) & 4) + ((l & 24) << 2) + ((l >> 1) & 3);
    const int row = ((l >> 3) & 1) + sel(isO, (l & 16) + ((l >> 4) & 2), (l & 32) + ((l >> 3) & 2));
    unsigned tlo = sel(kvt, rowK * 32 + (l & 1) * 16, row * 1024 + (l & 7) * 16) + so;
    PIN(oa); PIN(ob); PIN(oc); PIN(tlo);
    asm volatile("s_barrier" :: "s"(rw) : "memory");
    tv = __builtin_amdgcn_raw_buffer_load_b128(rw, tlo, 0, 0);
    SB();
    fr[0] = __builtin_amdgcn_raw_buffer_load_b32(rwr, oa, 0, 0); fi[0] = __builtin_amdgcn_raw_buffer_load_b32(rwi, oa, 0, 0);
    fr[1] = __builtin_amdgcn_raw_buffer_load_b32(rwr, ob, 0, 0); fi[1] = __builtin_amdgcn_raw_buffer_load_b32(rwi, ob, 0, 0);
    sr = ld16o(rwr, oc, 0); si = ld16o(rwi, oc, 0);
    st128(sent, rs, t * 16, OFF_UP + p * 8192);
    FENCE_S();
    {
      const int n = (4 * q + (w & 3)) * c16;
      const float rev = (float)n * sel(w < 4, 1.0f / 16.0f, 1.0f / 256.0f), sc = sel(w < 4, GAM, TWS);
      const float c = sc * cosr(rev), sn = sc * sinr(rev);
      unsigned* ct = lds + L_CT + (w >> 2) * 512 + l * 4 + (w & 3);
      ct[0] = sel(w < 4, pack2(c, sn), __builtin_bit_cast(unsigned, c));
      ct[256] = sel(w < 4, pack2(-sn, c), __builtin_bit_cast(unsigned, -sn));
    }
    { u32x2 m1, m2; make_r8<false>(l, s2, m1, m2); mm = u32x4{m1[0], m1[1], m2[0], m2[1]}; }
    PIN(mm);
    ad = addr_a(w, l);
    { const int sb = p >> 5; const bool o2 = (w >> 1) == 1;
      tdst = (w & 1) * 1024 + sel(w >= 4, OFF_WKV + p * 2048,
                 OFF_ST + (p & 3) * 256 + sel(o2, ((p >> 3) & 3) * 128 + sel(sb < 4, 32 + 16 * sb, 64 + 8 * sb) + ((p >> 1) & 2),
                                                  sel(sb < 4, 8 * sb, 16 * sb - 24) + ((p >> 2) & 7)) * 2048);
      tvo = sel(tile_on, c16 * 16 + sel(w < 2, q * 262144, sel(o2, ((q >> 1) + 4 * (q & 1)) * 2048, q * 256)), 0x7f000000);
      trb = L_QI + w * 256 + sel(w < 4, (c16 >> 2) * 32 + q * 8 + (l & 3) * 2, q * 64 + c16 * 2);
      PIN(tvo); PIN(trb); asm volatile("" : "+s"(tdst)); }
    asm volatile("s_waitcnt lgkmcnt(0)" : "+s"(ep));
    ep += 0x9E3779B97F4A7C15ull;
    tag_lo = (unsigned)ep; tag_hi = (unsigned)(ep >> 32);
    BAR();
    { const u32x4* ct = (const u32x4*)(lds + L_CT);
      F.re = ct[l]; F.im = ct[64 + l]; TF.c = __builtin_bit_cast(f32x4, ct[128 + l]); TF.s = __builtin_bit_cast(f32x4, ct[192 + l]); }
    SB();
    STAMP(30);
    asm volatile("" :: "v"(tc));
    PIN(xb);
    STAMP(31);
    re = __builtin_bit_cast(f32x4, xb << 16); im = __builtin_bit_cast(f32x4, xb & 0xffff0000u);
    pv = rdlane(re[0], 0);
    re -= pv; im -= pv;
  }
  const int aw = ad.aw, ar1 = ad.ar1, ar2 = ad.ar2, t16 = ad.t16;
  {
    u32x4 a = layernorm_pack(re, im);
    if constexpr (LAYER == 0) {
      PIN(a);
      asm volatile("s_waitcnt vmcnt(8)\n\ts_barrier" ::: "memory");
      if (t == 448) __builtin_amdgcn_raw_buffer_store_b64(u32x2{tag_lo, tag_hi}, rs, 0, OFF_FLG + p * FLAG_STRIDE, SC1);
      SB();
      STAMP(25);
    }
    f32x4 ore = z4, oim = z4;
    fft256(a, F, TF, F, ore, oim);
    u32x4 pk = pack4(ore, oim);
#pragma unroll
    for (int r = 0; r < 4; ++r) ldsw(aw + 640 * r, pk[r]);
    BAR();
    u32x2 b1 = ldsr<u32x2>(ar1), b2 = ldsr<u32x2>(ar2);
    f32x4 e1 = mf16(u32x2{mm[0], mm[1]}, b1, z4), e2 = mf16(u32x2{mm[2], mm[3]}, b2, z4);
    st128(u32x4{pack2(e1[0], e1[1]), pack2(e1[2], e1[3]), pack2(e2[0], e2[1]), pack2(e2[2], e2[3])}, rs, t16, OFF_U + LAYER * 2 * MB + p * 8192);
    SB();
    STAMP(9 + 12 * LAYER);
  }
  FRESH(); SB();
  unsigned goff = (t >> 1) * 8192 + p * 32 + (t & 1) * 16;
  const unsigned pfb = OFF_ST + (w - 4) * 262144, kvw = OFF_WKV + (w - 7) * 16384;
  u32x4 u;
  u32x2 fl;
  if constexpr (LAYER == 0) {
    *(u32x4*)(lds + L_QI + t * 4) = tv;
    const u32x2 h0 = tr_read(lds + trb), h1 = tr_read(lds + trb + sel(w < 4, 128, 32));
    st128(u32x4{h0[0], h0[1], h1[0], h1[1]}, rs, tvo, tdst);
    PIN(goff);
    SB();
    STAMP(27);
    u32x4 fg = {__builtin_amdgcn_perm(fi[0], fr[0], 0x05040100), __builtin_amdgcn_perm(fi[0], fr[0], 0x07060302),
                __builtin_amdgcn_perm(fi[1], fr[1], 0x05040100), __builtin_amdgcn_perm(fi[1], fr[1], 0x07060302)};
    unsigned sd = sr | (si << 16), sdo = sel((t & 126) == 0, (w + (l & 1)) * 4, 0x7f000000);
    PIN(fg); PIN(sd); PIN(sdo);
    DRAIN();
    asm volatile("s_barrier" ::: "memory");
    st128(fg, rs, t16, OFF_WR + p * 8192);
    __builtin_amdgcn_raw_buffer_store_b32(sd, rs, sdo, OFF_WS + p * 32, SC1);
    fl = flag_load(rs, t >> 1, 0);
    SB(); STAMP(6);
    if (w == 7) { ((u32x4*)(lds + L_CT))[256 + l] = mm; lds[L_FLAG] = 0; }
    *(u32x4*)(lds + L_XS + w * 768 + 512 + l * 4) = u32x4{(unsigned)aw, (unsigned)ar1, (unsigned)ar2, (unsigned)(L_R1 + w * 544 + l * 4) * 4};
    FENCE_S();
  }
  else { undef_tiles<8, 12>(pf); prefetch<8, 12>(pf, w >= 4, rs, l, pfb); }
  SB();
  const int qk = (p >> 3) & 3, sg0 = 16 * (p >> 5) + 2 * (p & 7);
  const Unit un = unit_of(qk, sg0, w);
  Dft I, P; Tw TP, TI;
#pragma unroll
  for (int r = 0; r < 4; ++r) { I.re[r] = F.re[r] ^ 0x80000000u; I.im[r] = F.im[r] ^ 0x00008000u; }
  P = I; TP.c = TF.c; TP.s = -TF.s;
  if (un.shift) { P = make_dft(l, 1.f, 1); TP = make_tw(l, 1.f, 1); }
  const float th = (float)un.g * (1.0f / 512.0f);
  float cu = 0.5f - 0.5f * sinr(th), cv = 1.0f - cu, ch = 0.5f * cosr(th);
  f32x4 ar, ai, dr, di, br, bi;
  const u32x4* wst = (const u32x4*)(lds + L_WST);
#define COEFS() do { u32x4 WA = wst[un.wa * 64 + l], WB = wst[(un.wa + 1) * 64 + l]; \
      f32x4 a_r = __builtin_bit_cast(f32x4, WA << 16), a_i = __builtin_bit_cast(f32x4, WA & 0xffff0000u); \
      f32x4 b_r = __builtin_bit_cast(f32x4, WB << 16), b_i = __builtin_bit_cast(f32x4, WB & 0xffff0000u);    \
      if (un.g == 0) {     \
        WA = wst[(8 + un.wa) * 64 + l]; WB = wst[(9 + un.wa) * 64 + l]; \
        a_r = 0.5f * (a_r + __builtin_bit_cast(f32x4, WA << 16)); a_i = 0.5f * (a_i - __builtin_bit_cast(f32x4, WA & 0xffff0000u)); \
        b_r = 0.5f * (b_r + __builtin_bit_cast(f32x4, WB << 16)); b_i = 0.5f * (b_i - __builtin_bit_cast(f32x4, WB & 0xffff0000u)); } \
      ar = cu * a_r + cv * b_r; ai = cu * a_i - cv * b_i; dr = cv * a_r + cu * b_r; di = cv * a_i - cu * b_i; \
      br = -ch * (a_i + b_i); bi = ch * (a_r - b_r); } while (0)
  if constexpr (LAYER == 1) {
    UNDEF(ar); UNDEF(ai); UNDEF(dr); UNDEF(di); UNDEF(br); UNDEF(bi);
    if (un.on) COEFS();
    PIN(ar); PIN(ai); PIN(dr); PIN(di); PIN(br); PIN(bi);
  }
  PIN(I.re); PIN(I.im); PIN(P.re); PIN(P.im); PIN(TP.c); PIN(TP.s); PIN(cu); PIN(cv); PIN(ch); PIN(goff);
  int bw = (L_R1 + (t & 1) * 1040 + (t >> 1)) * 4, bo1 = (L_R1 + un.t1 * 260 + l * 4) * 4, bo2 = (L_R1 + un.t2 * 260 + l * 4) * 4;
  PIN(bw); PIN(bo1); PIN(bo2);
  if constexpr (LAYER == 0) {
    unsigned so_ = sel(sg0 == 0, (t >> 1) * 32 + (2 * qk + (t & 1)) * 4, 0x7f000000);
    const int hp = t & 1, k2 = s2_of_pidx(t >> 1), four = (qk == 3) & hp, hm = sel(qk < 3, hp ^ 1, hp);
    unsigned pa = L_WST + hp * 512 + (t >> 1), pb = L_WST + hm * 512 + 256 + pidx_of_s2(-k2 - four);
    unsigned wo_ = (t >> 1) * 8192 + qk * 2048 + sg0 * 16 + (t & 1) * 16;
    PIN(so_); PIN(pa); PIN(pb); PIN(wo_);
    PIN(fl);
    flag_wait(rs, t >> 1, 0, fl, tag_lo, tag_hi);
    u = ld128(rs, goff, OFF_U, 1);
    STAMP(26);
    delay<DLY_F>();
    SB();
    u32x4 fg = ld128(rs, wo_, OFF_WR, 1);
    unsigned sd = __builtin_amdgcn_raw_buffer_load_b32(rs, so_, OFF_WS, SC1);
    { u32x4 sent = mk_sent();
      st128(sent, rs, t16, OFF_U + 2 * MB + p * 8192);
      st128(sent, rs, sel((w == 7) & (p < 128), l * 16, 0x7f000000), OFF_KV + p * 1024); }
    SB();
    u = poll128(rs, goff, OFF_U, u);
    fg = poll128(rs, wo_, OFF_WR, fg);
    lds[pa] = fg[0]; lds[pa + 1024] = fg[1]; lds[pb] = fg[3]; lds[pb + 1024] = fg[2];
    if (sg0 == 0) {
#pragma unroll 1
      for (unsigned spins = 0; __any(sd == SENT) && spins < SPIN_CAP; ++spins) {
        asm volatile("" ::: "memory");
        if (sd == SENT) sd = __builtin_amdgcn_raw_buffer_load_b32(rs, so_, OFF_WS, SC1);
      }
      lds[pb + 2048 - 256] = fg[0]; lds[pa + 2048 + 256] = fg[3];
      lds[L_WST + 3072 + sel(hp != 0, t >> 1, 256 + pidx_of_s2(-k2))] = sd;
    }
  } else {
    NEW_PAGE();
    delay<DLY_B>();
    SB();
    u = ld128(rs, goff, OFF_U + 2 * MB, 1);
    u = poll128(rs, goff, OFF_U + 2 * MB, u);
  }
  STAMP(1 + 12 * LAYER);
#pragma unroll
  for (int i = 0; i < 4; ++i) ldsw(bw + i * 1040, u[i]);
  if constexpr (LAYER == 0) DRAIN();
  BAR();
  STAMP(10 + 12 * LAYER);
  if constexpr (LAYER == 0) {
    if (t == 448 && p == 0) __builtin_amdgcn_raw_buffer_store_b64(u32x2{tag_lo, tag_hi}, rs, 0, OFF_EPO, SC1);
    undef_tiles<0, 16>(pf);
    prefetch<8, 16>(pf, w >= 6, rs, l, kvw);
    prefetch<0, 3>(pf, !un.on, rs, l, pfb);
    prefetch<3, 8>(pf, !un.on & (w < 6), rs, l, pfb);
  }
  else { undef_tiles<12, 16>(pf); prefetch<12, 16>(pf, !un.on, rs, l, pfb); }
  TI.c = TF.c; TI.s = -TF.s;
  if (un.on) {
    u32x4 own = ldsr<u32x4>(bo1), par = ldsr<u32x4>(bo2);
    if constexpr (LAYER == 0) COEFS();
    f32x4 Ar = z4, Ai = z4, Br = z4, Bi = z4;
    fft256(own, F, TF, F, Ar, Ai);
    fft256(par, P, TP, I, Br, Bi);
    f32x4 yr = ar * Ar - ai * Ai + br * Br + bi * Bi;
    f32x4 yi = ar * Ai + ai * Ar + bi * Br - br * Bi;
    f32x4 cr = dr * Br + di * Bi - br * Ar + bi * Ai;
    f32x4 ci = dr * Bi - di * Br + br * Ai + bi * Ar;
    f32x4 o1r = z4, o1i = z4, o2r = z4, o2i = z4;
    fft256(pack4(cr, ci), F, TF, F, o2r, o2i);
    fft256(pack4(yr, yi), I, TI, I, o1r, o1i);
    ldsw(bo2 + 8320, pack4(o2r, o2i));
    ldsw(bo1 + 8320, pack4(o1r, o1i));
  }
  BAR();
  {
    u32x4 o;
#pragma unroll
    for (int i = 0; i < 4; ++i) o[i] = ldsr<unsigned>(bw + 8320 + i * 1040);
    st128(o, rs, t16, OFF_UP + LAYER * 2 * MB + p * 8192);
  }
  SB();
  STAMP(2 + 12 * LAYER);
  const u32x4* wkl = (const u32x4*)(lds + L_WKV);
#define KV_TILES() do { if (w < 2) { _Pragma("unroll") for (int i = 0; i < 8; ++i) { \
      const u32x4 a_ = wkl[(w * 8 + i) * 128 + l], b_ = wkl[(w * 8 + i) * 128 + 64 + l]; \
      pf[8 + i] = __builtin_shufflevector(a_, b_, 0, 1, 2, 3, 4, 5, 6, 7); } } } while (0)
  if constexpr (LAYER == 0) {
    if (w >= 6) {
#pragma unroll
      for (int i = 0; i < 8; ++i) {
        ((u32x4*)(lds + L_WKV))[((w - 6) * 8 + i) * 128 + l] = __builtin_shufflevector(pf[8 + i], pf[8 + i], 0, 1, 2, 3);
        ((u32x4*)(lds + L_WKV))[((w - 6) * 8 + i) * 128 + 64 + l] = __builtin_shufflevector(pf[8 + i], pf[8 + i], 4, 5, 6, 7);
      }
    }
  }
  else { KV_TILES(); prefetch<12, 16>(pf, un.on && w >= 4, rs, l, pfb); }
  SB();
  FRESH();
  u32x2 m1, m2; make_r8<true>(l, s2, m1, m2);
  int sg = (L_PART + (2 * q) * 264 + 16 * w + c16) * 4, g2 = (L_PART + (2 * q) * 264 + sel(16 * w + c16 != 0, 256 - 16 * w - c16, 128)) * 4;
  if constexpr (LAYER == 1) { const float d = rdlane(re[0], 0); re -= d; im -= d; pv += d; }
  int cr_ = (L_PART + w * 264 + c16 + 64 * q) * 4, lfw = ((l >> 3) * 64 + ((l >> 1) & 3) * 16 + ((2 * w + (l & 1)) ^ (l & 6))) * 16 + L_LF * 4;
  int lfr = (q * 16 + (c16 ^ (2 * q))) * 16 + L_LF * 4, sidx = sel((l & 1) != 0, 0xeeee, 0x4444);
  unsigned kvo = sel(w == 0, ((p >> 2) * 256 + ((c16 >> 3) * 32 + (p & 3) * 8 + 2 * q + (l & 1)) * 4 + ((c16 >> 1) & 3)) * 4, 65536 + (p * 64 + c16 * 4 + q) * 4);
  unsigned psel = sel(w != 0, 0x03020100u, sel((l & 1) != 0, 0x03020706u, 0x05040100u));
  f32x4 kacc = mk_zero4();
  PIN(lfr); PIN(sidx); PIN(kvo); PIN(psel);
  PIN(m1); PIN(m2); PIN(sg); PIN(g2); PIN(cr_); PIN(lfw); PIN(re); PIN(im);
  if constexpr (LAYER == 0) NEW_PAGE();
  if (LAYER == 1 || w < 4) delay<DLY_C>();
  SB();
  u = ld128(rs, goff, OFF_UP + LAYER * 2 * MB, 1);
  u = poll128(rs, goff, OFF_UP + LAYER * 2 * MB, u);
  STAMP(3 + 12 * LAYER);
  {
    f32x4 e1 = mf16(m1, u32x2{u[0], u[1]}, z4), e2 = mf16(m2, u32x2{u[2], u[3]}, z4);
    SB();
    ldsw(sg, pack2(e1[0], e1[1])); ldsw(sg + 1056, pack2(e1[2], e1[3]));
    ldsw(g2, pack2(e2[0], e2[1])); ldsw(g2 + 1056, pack2(e2[2], e2[3]));
    BAR();
    STAMP(11 + 12 * LAYER);
    u32x4 a;
#pragma unroll
    for (int r = 0; r < 4; ++r) a[r] = ldsr<unsigned>(cr_ + 64 * r);
    if constexpr (LAYER == 0) { undef_tiles<8, 16>(pf); KV_TILES(); prefetch<3, 6>(pf, (w >= 6) | (un.on & (w >= 4)), rs, l, pfb); }
    fft256(a, I, TI, I, re, im);
    if constexpr (LAYER == 0) { PIN(re); PIN(im); prefetch<6, 8>(pf, PFV == 2 ? w >= 4 : (w >= 6) | (un.on & (w >= 4)), rs, l, pfb); if (PFV == 0) prefetch<0, 3>(pf, un.on & (w >= 4), rs, l, pfb); }
    ldsw(lfw, layernorm_pack(re, im));
#pragma unroll
    for (int i = 0; i < 16; ++i) asm volatile("" :: "v"(pf[i]));
    BAR();
    if (w < 2) {
      u32x4 af[8];
#pragma unroll
      for (int ks = 0; ks < 8; ++ks) af[ks] = ldsr<u32x4>(lfr + ks * 1024);
      SB();
#pragma unroll
      for (int ks = 0; ks < 8; ++ks) kacc = smf64(af[ks], pf[8 + ks], kacc, sidx);
      const unsigned pk = pack2(kacc[0] + kacc[1], kacc[2] + kacc[3]);
      const unsigned nb = __builtin_amdgcn_update_dpp(0, pk, 0xB1, 0xf, 0xf, true);
      __builtin_amdgcn_raw_buffer_store_b32(__builtin_amdgcn_perm(nb, pk, psel), rs, kvo, OFF_KV + LAYER * KV_STRIDE, SC1);
    }
    SB();
  }
}

__global__ void __launch_bounds__(512, 2) k_main(const unsigned* __restrict__ x, const u16* __restrict__ gwr, const u16* __restrict__ gwi,
                                                 const u16* __restrict__ gwq, const u16* __restrict__ gwk, const u16* __restrict__ gwv,
                                                 const u16* __restrict__ gwo, char* ws, unsigned* __restrict__ out) {
  __shared__ __attribute__((aligned(16))) unsigned lds[L_END + 32];
  unsigned tc;
  {
    unsigned long long pc; unsigned len;
    asm volatile("s_getpc_b64 %0\n.Lmqa_pc:\n\ts_mov_b32 %1, .Lmqa_finpad-.Lmqa_pc+64" : "=s"(pc), "=s"(len));
    const unsigned tx = threadIdx.x, bi = blockIdx.x >> 3;
    tc = __builtin_amdgcn_raw_buffer_load_b32(__builtin_amdgcn_make_buffer_rsrc((void*)pc, 0, len, 0x00020000),
                                              sel(tx < 16, tx * 4 + (bi & 3), (tx - 14) * 32 + bi) * 128, 0, 0);
    SB();
  }
  asm volatile("" :: "s"(x), "s"(gwr), "s"(gwi), "s"(gwq), "s"(gwk), "s"(gwv), "s"(gwo), "s"(ws), "s"(out));
  const int w0 = __builtin_amdgcn_readfirstlane(threadIdx.x >> 6), p0 = pidx_of_block(blockIdx.x);
  int t = threadIdx.x, w, p, l, c16, q, s2;
  const rsrc_t rs = __builtin_amdgcn_make_buffer_rsrc(ws, 0, WS_USED, 0x00020000);
  const Ptrs g = {x, gwr, gwi, gwq, gwk, gwv, gwo, ws};
  STAMP(0);
  u32x8 pf[16];
  f32x4 re, im; float pv;
  spectral<0>(g, rs, lds, w0, p0, re, im, pv, Dft{}, Tw{}, u32x4{}, pf, tc, AddrA{} STAMP_ARGS);
  rsrc_t ro = __builtin_amdgcn_make_buffer_rsrc(out + (s2_of_pidx(p0) + 256 * w0) * 256, 0, 1024, 0x00020000);
  asm volatile("" : "+s"(ro));
#pragma unroll 1
  for (int layer = 0;; ++layer) {
    FRESH();
    const unsigned kvb = OFF_KV + layer * KV_STRIDE;
    float* rows = (float*)(lds + L_R1);
    Dft F; Tw TF;
    if ((w & 6) == 2) delay<DLY_E>();
    { const float d = sel(layer == 0, rdlane(re[0], 0), -rdlane(pv, 0)); re -= d; im -= d; pv = rdlane(pv + d, 0); }
    SB();
    STAMP(4 + 12 * layer);
    {
      int d = l & 31, h = l >> 5, b = w - 4;
      bool A = w < 4;
      unsigned in[8];
      in[0] = l * 16;
      in[1] = sel(A, w * 16384 + l * 16, (q * 16 + (c16 ^ (2 * q))) * 16);
      in[2] = sel(A, 65536 + w * 16384 + (q & 1) * 512 + h * 256 + c16 * 16,
                     (((b & 1) + 4 * q + 16 * (b >> 1) + 32 * (c16 >> 3)) * 8 + (c16 & 7)) * 2);
      in[3] = sel(A, w * 3072, (b >> 1) * 1024 + (b & 1) * 8) + l * 16;
      in[4] = sel(A, sel(c16 == 0, 0x3f803f80, 0), d * 16);
      in[5] = l * 16 + b * 4;
      in[6] = (2 * q * 544 + (c16 & 1) * 272 + (b * 16 + (c16 >> 1)) * 4) * 4;
      in[7] = (l & 1) ? 0xeeee : 0x4444;
      unsigned kv1 = kvb + 4096, kv2 = kvb + 8192, kv3 = kvb + 12288, cnt, cls = 0x207;
      unsigned stb = OFF_ST + b * 262144 + 7 * 4096;
      unsigned po1 = layer == 0 ? OFF_UP + 2 * MB + p * 8192 + b * 2048 : 0;
      unsigned po2 = (b == 0 && p < 128) ? OFF_KV + KV_STRIDE + p * 1024 : 0;
      unsigned long long bad, sc = 0x3eb8aa3b3eb8aa3bull;
      unsigned long long sc_[ATT_NST + 1];
      *(f32x4*)(lds + L_XS + w * 768 + l * 4) = re; *(f32x4*)(lds + L_XS + w * 768 + 256 + l * 4) = im;
      const unsigned xso = (L_XS + w * 768) * 4;
      STAMP(5 + 12 * layer);
      asm volatile(ATT_ASM
                   : [cnt] "=&s"(cnt), [stb] "+s"(stb), [bad] "=&s"(bad), ATT_RING(pf), ATT_IN(in), ATT_OUT(F.re, F.im, TF.c, TF.s) ATT_STOUT
                   : [rs] "s"(rs), [kv0] "s"(kvb), [kv1] "s"(kv1), [kv2] "s"(kv2), [kv3] "s"(kv3), [w] "s"(w),
                     [po1] "s"(po1), [po2] "s"(po2), [sc] "s"(sc), [cls] "s"(cls), [dly] "i"(DLY_D), [xso] "s"(xso)
                   : ATT_CLOB);
      l = in[0]; __builtin_assume(l >= 0 && l < 64);
      w = w0; p = p0; asm volatile("" : "+s"(w)); asm volatile("" : "+s"(p)); t = w * 64 + l; c16 = l & 15; q = l >> 4; s2 = s2_of_pidx(p);
      STAMP(7 + 12 * layer);
    }
    const u32x4 mm = __builtin_shufflevector(pf[10], pf[10], 0, 1, 2, 3);
    re = __builtin_bit_cast(f32x4, __builtin_shufflevector(pf[10], pf[10], 4, 5, 6, 7)); im = __builtin_bit_cast(f32x4, __builtin_shufflevector(pf[11], pf[11], 0, 1, 2, 3));
    const int rr = pf[11][7];
    f32x4 ore = __builtin_bit_cast(f32x4, __builtin_shufflevector(pf[12], pf[12], 0, 1, 2, 3)), oim = __builtin_bit_cast(f32x4, __builtin_shufflevector(pf[12], pf[12], 4, 5, 6, 7));
    const unsigned flg = pf[13][0];
    const AddrA ad = {(int)pf[11][4], (int)pf[11][5], (int)pf[11][6], t * 16};
    if (__builtin_expect(__builtin_amdgcn_readfirstlane(flg) != 0, 0)) {
      BAR();
      if (t == 0) lds[L_FLAG] = 0;
      int d = l & 31, h = l >> 5, sidx = (l & 1) ? 0xeeee : 0x4444;
      unsigned ko = w * 8192 + l * 16;
      unsigned vo = 65536u + w * 8192 + h * 256 + c16 * 16, cst = d == 16 ? 0x3f803f80u : 0u;
      const u32x4 cst4 = {cst, cst, cst, cst};
#pragma unroll 1
      for (int i = 0; i < 8; ++i) {
        poll128(rs, ko, kvb + i * 1024, mk_sent());
        poll128(rs, vo, kvb + i * 1024, mk_sent());
        poll128(rs, vo + 512, kvb + i * 1024, mk_sent());
      }
      f32x4 oacc[4];
#pragma unroll
      for (int i = 0; i < 4; ++i) oacc[i] = mk_zero4();
      float* part = (float*)(lds + L_PART);
      float* mx = rows;
#pragma unroll 1
      for (int s = 0; s < 8; ++s) {
        u32x4 qf = ((const u32x4*)(lds + L_QI))[s * 64 + l];
        const f32x16 z = {0};
        float m = -__builtin_inff();
#pragma unroll 1
        for (int i = 0; i < 8; ++i) {
          f32x16 sc = QKM(ld128(rs, ko, kvb + i * 1024, 1), qf, z);
#pragma unroll
          for (int j = 0; j < 16; ++j) m = __builtin_fmaxf(m, sc[j]);
        }
        mx[t] = m;
        BAR();
#pragma unroll
        for (int v = 0; v < 16; ++v) m = __builtin_fmaxf(m, mx[v * 32 + (l & 31)]);
        f32x4 zz = mk_zero4();
        f32x16 o = __builtin_shufflevector(zz, zz, 0, 1, 2, 3, 0, 1, 2, 3, 0, 1, 2, 3, 0, 1, 2, 3);
#pragma unroll 1
        for (int i = 0; i < 8; ++i) {
          f32x16 sc = QKM(ld128(rs, ko, kvb + i * 1024, 1), qf, z);
#pragma unroll
          for (int j = 0; j < 16; ++j) sc[j] = __builtin_amdgcn_exp2f(sc[j] - m);
          u32x4 v0 = ld128(rs, vo, kvb + i * 1024, 1), v1 = ld128(rs, vo + 512, kvb + i * 1024, 1);
          o = PVM(d < 16 ? v0 : cst4, lo8(sc), o);
          o = PVM(d < 16 ? v1 : cst4, hi8(sc), o);
        }
#pragma unroll
        for (int r = 0; r < 9; ++r) part[r * 512 + t] = o[r];
        BAR();
        float so = 0, sl = 0;
#pragma unroll
        for (int v = 0; v < 8; ++v) { so += part[w * 512 + v * 64 + l]; sl += part[4096 + v * 64 + (l & 31)]; }
        ((u16*)(lds + L_OFR))[l * 8 + w] = (u16)pack2(so * __builtin_amdgcn_rcpf(sl), 0.f);
        BAR();
        u32x4 of = ((const u32x4*)(lds + L_OFR))[l];
        unsigned n0 = OFF_ST + (w >> 1) * 262144 + (out_slot0(s) + (w & 1) * 4) * 2048;
#pragma unroll
        for (int i = 0; i < 4; ++i) oacc[i] = smf64(of, ldtile(rs, l * 16, n0 + i * 2048), oacc[i], sidx);
      }
      rows_write(rows, l, w, oacc);
      BAR();
      ore = ldsr<f32x4>(rr); oim = ldsr<f32x4>(rr + 1088);
    }
    re += ore; im += oim;
    STAMP(8 + 12 * layer);
    if (layer == 1) break;
    spectral<1>(g, rs, lds, w0, p0, re, im, pv, F, TF, mm, pf, 0, ad STAMP_ARGS);
  }
  asm volatile(".Lmqa_finpad:\n\ts_cbranch_execz .Lmqa_padret");
  SB();
  { const u32x4 xb = pack4(re, im);
#pragma unroll
    for (int r = 0; r < 4; ++r) __builtin_amdgcn_raw_buffer_store_b32(xb[r], ro, (c16 + 64 * q + 16 * r) * 4, 0, 0); }
  STAMP(24);
}

extern "C" void kernel_launch(void* const* d_in, const int* in_sizes, int n_in, void* d_out, int out_size, void* d_ws,
                              size_t ws_size, hipStream_t stream) {
  k_main<<<256, 512, 0, stream>>>((const unsigned*)d_in[0], (const u16*)d_in[1], (const u16*)d_in[2], (const u16*)d_in[4],
                                  (const u16*)d_in[5], (const u16*)d_in[6], (const u16*)d_in[7], (char*)d_ws + WS_BASE, (unsigned*)d_out);
}
```
